```python
import torch

def _softmax(x, axis=-1):
    return torch.softmax(x, dim=axis)
def _gelu(x):
    return torch.nn.functional.gelu(x, approximate="tanh")
def _silu(x):
    return torch.nn.functional.silu(x)
def _sigmoid(x):
    return torch.sigmoid(x)
def _layernorm(x, axis=-1, eps=1e-5):
    m = x.mean(dim=axis, keepdim=True)
    v = x.var(dim=axis, keepdim=True, unbiased=False)
    return (x - m) / torch.sqrt(v + eps)
def _scatter_add(tgt, idx, src, axis=0):
    return tgt.clone().index_add_(axis, idx, src)
def _segment_sum(src, seg, n):
    out = torch.zeros((n,) + src.shape[1:], dtype=src.dtype)
    return out.index_add_(0, seg, src)
def _pad(x, pw):
    flat = tuple(v for pair in reversed(pw) for v in pair)
    return torch.nn.functional.pad(x, flat)

kernel_name = 'sequential_preLN__mha_block__mixer_s_mlp__prompt'

def setup_inputs():

    _w = (lambda a: (lambda t: t.to(torch.bfloat16) if t.is_floating_point() else t)(torch.as_tensor(a)))
    def _A_setup():
        B, S, D, H = 1, 2048, 576, 32
        return {
            "x": _w((lambda *s: torch.randn(*s, dtype=torch.bfloat16) * 0.05)(B, S, D)),
            "wqkv": _w((lambda *s: torch.randn(*s, dtype=torch.bfloat16) * 0.05)(D, 3 * D)),
            "wo": _w((lambda *s: torch.randn(*s, dtype=torch.bfloat16) * 0.05)(D, D)),
        }
    _A_in = _A_setup()

    def _B_setup():
        B, S, D, F = 1, 2048, 576, 4608
        return {
            "x":  _w((lambda *s: torch.randn(*s, dtype=torch.bfloat16) * 0.05)(B, S, D)),
            "w1": _w((lambda *s: torch.randn(*s, dtype=torch.bfloat16) * 3.2)(D, D) * (D ** -0.5)),
            "w2": _w((lambda *s: torch.randn(*s, dtype=torch.bfloat16) * 3.2)(D, D) * (D ** -0.5)),
            "w3": _w((lambda *s: torch.randn(*s, dtype=torch.bfloat16) * 3.2)(D, F) * (D ** -0.5)),
            "w4": _w((lambda *s: torch.randn(*s, dtype=torch.bfloat16) * 3.2)(F, D) * (F ** -0.5)),
        }
    _B_in = _B_setup()

    return {
        "A_x": _A_in["x"],
        "A_wqkv": _A_in["wqkv"],
        "A_wo": _A_in["wo"],
        "B_x": _B_in["x"],
        "B_w1": _B_in["w1"],
        "B_w2": _B_in["w2"],
        "B_w3": _B_in["w3"],
        "B_w4": _B_in["w4"],
    }

def reference(A_x, A_wqkv, A_wo, B_x, B_w1, B_w2, B_w3, B_w4):
    def _A_ref(x, wqkv, wo):
        B, S, D = x.shape
        H = 32
        Dh = D // H
        qkv = (lambda x, *d: x.permute(*d))((x @ wqkv).reshape(B, S, 3, H, Dh), 2, 0, 3, 1, 4)
        q, k, v = qkv[0], qkv[1], qkv[2]
        scores = torch.einsum("bhqd,bhkd->bhqk", q, k) * (Dh ** -0.5)
        p = _softmax(scores, axis=-1)
        o = torch.einsum("bhqk,bhkd->bhqd", p, v)
        return (lambda x, *d: x.permute(*d))(o, 0, 2, 1, 3).reshape(B, S, D) @ wo

    def _B_ref(x, w1, w2, w3, w4):
        B, S, D = x.shape
        h = _gelu(_layernorm(x, axis=-1) @ w1)
        c = D // 4
        g0 = _pad(h[:, :, 0*c:1*c], ((0, 0), (1, 0), (0, 0)))[:, :S, :]
        g1 = _pad(h[:, :, 1*c:2*c], ((0, 0), (0, 1), (0, 0)))[:, 1:, :]
        g2 = _pad(h[:, :, 2*c:3*c], ((0, 0), (2, 0), (0, 0)))[:, :S, :]
        g3 = _pad(h[:, :, 3*c:   ], ((0, 0), (0, 2), (0, 0)))[:, 2:, :]
        hs = torch.concatenate([g0, g1, g2, g3], axis=-1) @ w2
        x1 = x + hs
        h2 = _gelu(_layernorm(x1, axis=-1) @ w3) @ w4
        return x1 + h2

    x = A_x
    h = x + _A_ref(_layernorm(x, axis=-1), A_wqkv, A_wo)
    h2 = h + _B_ref(_layernorm(h, axis=-1), B_w1, B_w2, B_w3, B_w4)
    return h2
```

```cpp
#include <hip/hip_runtime.h>
#include <hip/hip_bf16.h>
#include <stdint.h>

static constexpr int SEQ = 2048, DM = 576, NH = 32, DH = 18, FF = 4608;
static constexpr int DP = 32;

using bf16x8 = __attribute__((ext_vector_type(8))) short;
using s16x4 = __attribute__((ext_vector_type(4))) short;
using f32x4 = __attribute__((ext_vector_type(4))) float;
using f32x16 = __attribute__((ext_vector_type(16))) float;
using u32x4 = __attribute__((ext_vector_type(4))) unsigned;
typedef short v4i16_t __attribute__((ext_vector_type(4)));
typedef unsigned short u16;

__device__ __forceinline__ float bf2f(u16 v) { return __uint_as_float(((unsigned)v) << 16); }
__device__ __forceinline__ u16 f2bf(float f) {
    unsigned u = __float_as_uint(f);
    u += 0x7FFF + ((u >> 16) & 1);
    return (u16)(u >> 16);
}
__device__ __forceinline__ float rbf(float f) { return bf2f(f2bf(f)); }

__device__ __forceinline__ float gelu_tanh(float x) {
    const float u = 0.7978845608028654f * (x + 0.044715f * x * x * x);
    const float e = __expf(2.f * u);
    const float t = 1.f - 2.f / (e + 1.f);
    return 0.5f * x * (1.f + t);
}

struct GemmArgs {
    const u16* A;
    const u16* B;
    u16* C;
    const u16* R1;
    const u16* R2;
    u16* Y;
    int N;
    int K;
};

enum { PRO_NONE = 0, PRO_LN = 1, PRO_LN2 = 2, PRO_SHIFT = 3 };
enum { EPI_QKV = 0, EPI_RES = 1, EPI_GELU = 2, EPI_RES2 = 3 };

template <int BM, int BN, int PRO, int EPI>
__global__ __launch_bounds__(256) void gemm_k(GemmArgs a) {
    constexpr int BK = 64;
    constexpr int ASTR = BK + 8;
    constexpr int BSTR = BN + 8;
    constexpr int WM = BM / 2, WN = BN / 2;
    constexpr int TM = WM / 16, TN = WN / 16;
    constexpr int ACH = BM * BK / 8 / 256;
    constexpr int BCH = (BK * BN / 8 + 255) / 256;
    constexpr int BTOT = BK * BN / 8;

    __shared__ __attribute__((aligned(16))) u16 sA[2][BM * ASTR];
    __shared__ __attribute__((aligned(16))) u16 sB[2][BK * BSTR];
    __shared__ float sStat[BM * 4];

    const int tid = threadIdx.x, lane = tid & 63, wid = tid >> 6;
    const int wm = wid >> 1, wn = wid & 1;
    const int m0 = blockIdx.x * BM, n0 = blockIdx.y * BN;
    const int K = a.K, N = a.N;

    if constexpr (PRO == PRO_LN || PRO == PRO_LN2) {
        const int sub = lane & 7, rr = lane >> 3;
        for (int it = 0; it < BM / 32; ++it) {
            const int row = wid * (BM / 4) + it * 8 + rr;
            const u16* src = a.A + (size_t)(m0 + row) * DM;
            float v[72];
            float s = 0.f;
#pragma unroll
            for (int c = 0; c < 9; ++c) {
                bf16x8 t = *reinterpret_cast<const bf16x8*>(src + (c * 8 + sub) * 8);
#pragma unroll
                for (int j = 0; j < 8; ++j) { v[c * 8 + j] = bf2f((u16)t[j]); s += v[c * 8 + j]; }
            }
            s += __shfl_xor(s, 1); s += __shfl_xor(s, 2); s += __shfl_xor(s, 4);
            const float mean = s * (1.f / DM);
            float q = 0.f;
#pragma unroll
            for (int j = 0; j < 72; ++j) { const float d = v[j] - mean; q += d * d; }
            q += __shfl_xor(q, 1); q += __shfl_xor(q, 2); q += __shfl_xor(q, 4);
            const float mb = rbf(mean);
            const float sd = rbf(sqrtf(rbf(rbf(q * (1.f / DM)) + 1e-5f)));
            float m2b = 0.f, sd2 = 1.f;
            if constexpr (PRO == PRO_LN2) {
                float s2 = 0.f;
#pragma unroll
                for (int j = 0; j < 72; ++j) { v[j] = rbf(rbf(v[j] - mb) / sd); s2 += v[j]; }
                s2 += __shfl_xor(s2, 1); s2 += __shfl_xor(s2, 2); s2 += __shfl_xor(s2, 4);
                const float mean2 = s2 * (1.f / DM);
                float q2 = 0.f;
#pragma unroll
                for (int j = 0; j < 72; ++j) { const float d = v[j] - mean2; q2 += d * d; }
                q2 += __shfl_xor(q2, 1); q2 += __shfl_xor(q2, 2); q2 += __shfl_xor(q2, 4);
                m2b = rbf(mean2);
                sd2 = rbf(sqrtf(rbf(rbf(q2 * (1.f / DM)) + 1e-5f)));
                if (blockIdx.y == 0) {
#pragma unroll
                    for (int c = 0; c < 9; ++c) {
                        bf16x8 t;
#pragma unroll
                        for (int j = 0; j < 8; ++j) t[j] = (short)f2bf(v[c * 8 + j]);
                        *reinterpret_cast<bf16x8*>(a.Y + (size_t)(m0 + row) * DM + (c * 8 + sub) * 8) = t;
                    }
                }
            }
            if (sub == 0) { sStat[row * 4 + 0] = mb; sStat[row * 4 + 1] = sd; sStat[row * 4 + 2] = m2b; sStat[row * 4 + 3] = sd2; }
        }
        __syncthreads();
    }

    f32x4 acc[TM][TN];
#pragma unroll
    for (int i = 0; i < TM; ++i)
#pragma unroll
        for (int j = 0; j < TN; ++j) acc[i][j] = f32x4{0.f, 0.f, 0.f, 0.f};

    bf16x8 ra[ACH], rb[BCH];

    auto load_tiles = [&](int k0) {
#pragma unroll
        for (int i = 0; i < ACH; ++i) {
            const int idx = tid + i * 256, row = idx >> 3, ch = idx & 7;
            const int col = k0 + ch * 8;
            if constexpr (PRO == PRO_SHIFT) {
                const int grp = col / 144;
                const int sh = (grp == 0) ? -1 : (grp == 1) ? 1 : (grp == 2) ? -2 : 2;
                const int sr = m0 + row + sh;
                if (sr >= 0 && sr < SEQ) ra[i] = *reinterpret_cast<const bf16x8*>(a.A + (size_t)sr * K + col);
                else ra[i] = bf16x8{0, 0, 0, 0, 0, 0, 0, 0};
            } else {
                ra[i] = *reinterpret_cast<const bf16x8*>(a.A + (size_t)(m0 + row) * K + col);
            }
        }
#pragma unroll
        for (int i = 0; i < BCH; ++i) {
            const int idx = tid + i * 256;
            if (BTOT % 256 == 0 || idx < BTOT) {
                const int row = idx / (BN / 8), ch = idx % (BN / 8);
                rb[i] = *reinterpret_cast<const bf16x8*>(a.B + (size_t)(k0 + row) * N + n0 + ch * 8);
            }
        }
    };
    auto store_tiles = [&](int buf) {
#pragma unroll
        for (int i = 0; i < ACH; ++i) {
            const int idx = tid + i * 256, row = idx >> 3, ch = idx & 7;
            bf16x8 t = ra[i];
            if constexpr (PRO == PRO_LN || PRO == PRO_LN2) {
                const float mb = sStat[row * 4 + 0], sd = sStat[row * 4 + 1];
#pragma unroll
                for (int j = 0; j < 8; ++j) {
                    float y = rbf(rbf(bf2f((u16)t[j]) - mb) / sd);
                    if constexpr (PRO == PRO_LN2) y = rbf(rbf(y - sStat[row * 4 + 2]) / sStat[row * 4 + 3]);
                    t[j] = (short)f2bf(y);
                }
            }
            *reinterpret_cast<bf16x8*>(&sA[buf][row * ASTR + ch * 8]) = t;
        }
#pragma unroll
        for (int i = 0; i < BCH; ++i) {
            const int idx = tid + i * 256;
            if (BTOT % 256 == 0 || idx < BTOT) {
                const int row = idx / (BN / 8), ch = idx % (BN / 8);
                *reinterpret_cast<bf16x8*>(&sB[buf][row * BSTR + ch * 8]) = rb[i];
            }
        }
    };

    const int nk = K / BK;
    load_tiles(0);
    store_tiles(0);
    __syncthreads();
    for (int kt = 0; kt < nk; ++kt) {
        const int buf = kt & 1;
        if (kt + 1 < nk) load_tiles((kt + 1) * BK);
#pragma unroll
        for (int kk = 0; kk < 2; ++kk) {
            bf16x8 af[TM], bfr[TN];
#pragma unroll
            for (int i = 0; i < TM; ++i)
                af[i] = *reinterpret_cast<const bf16x8*>(&sA[buf][(wm * WM + i * 16 + (lane & 15)) * ASTR + kk * 32 + (lane >> 4) * 8]);
#pragma unroll
            for (int j = 0; j < TN; ++j) {
                const int g = lane >> 4, q = (lane & 15) >> 2, p = lane & 3;
                const u16* p0 = &sB[buf][(kk * 32 + 8 * g + q) * BSTR + wn * WN + j * 16 + 4 * p];
                s16x4 lo = __builtin_bit_cast(s16x4, __builtin_amdgcn_ds_read_tr16_b64_v4i16((__attribute__((address_space(3))) v4i16_t*)p0));
                s16x4 hi = __builtin_bit_cast(s16x4, __builtin_amdgcn_ds_read_tr16_b64_v4i16((__attribute__((address_space(3))) v4i16_t*)(p0 + 4 * BSTR)));
                bfr[j] = bf16x8{lo[0], lo[1], lo[2], lo[3], hi[0], hi[1], hi[2], hi[3]};
            }
#pragma unroll
            for (int i = 0; i < TM; ++i)
#pragma unroll
                for (int j = 0; j < TN; ++j)
                    acc[i][j] = __builtin_amdgcn_mfma_f32_16x16x32_bf16(af[i], bfr[j], acc[i][j], 0, 0, 0);
        }
        if (kt + 1 < nk) store_tiles(buf ^ 1);
        __syncthreads();
    }

#pragma unroll
    for (int i = 0; i < TM; ++i)
#pragma unroll
        for (int j = 0; j < TN; ++j)
#pragma unroll
            for (int r = 0; r < 4; ++r) {
                const int m = m0 + wm * WM + i * 16 + (lane >> 4) * 4 + r;
                const int n = n0 + wn * WN + j * 16 + (lane & 15);
                const float v = rbf(acc[i][j][r]);
                if constexpr (EPI == EPI_QKV) {
                    const int which = n / DM, rem = n % DM, hh = rem / DH, d = rem % DH;
                    u16* base = a.C + (size_t)which * (NH * SEQ * DP);
                    if (which < 2) base[((size_t)hh * SEQ + m) * DP + d] = f2bf(v);
                    else base[((size_t)hh * DP + d) * SEQ + m] = f2bf(v);
                } else if constexpr (EPI == EPI_RES) {
                    a.C[(size_t)m * N + n] = f2bf(bf2f(a.R1[(size_t)m * N + n]) + v);
                } else if constexpr (EPI == EPI_GELU) {
                    a.C[(size_t)m * N + n] = f2bf(gelu_tanh(v));
                } else {
                    const float t = rbf(bf2f(a.R1[(size_t)m * N + n]) + v);
                    a.C[(size_t)m * N + n] = f2bf(bf2f(a.R2[(size_t)m * N + n]) + t);
                }
            }
}

__global__ void pad_qkv_k(u16* qkv) {
    const int idx = blockIdx.x * 256 + threadIdx.x;
    const int hh = idx / SEQ, s = idx % SEQ;
    u16* Q = qkv;
    u16* Kp = qkv + (size_t)NH * SEQ * DP;
    u16* Vt = qkv + (size_t)2 * NH * SEQ * DP;
    for (int d = DH; d < DP; ++d) {
        Q[((size_t)hh * SEQ + s) * DP + d] = 0;
        Kp[((size_t)hh * SEQ + s) * DP + d] = 0;
        Vt[((size_t)hh * DP + d) * SEQ + s] = (d == DH) ? (u16)0x3F80 : (u16)0;
    }
}

static constexpr int KC = 128;
static constexpr int KSTR = 40;
static constexpr int VSTR = KC + 8;

__global__ __launch_bounds__(256) void attn_k(const u16* __restrict__ qkv, u16* __restrict__ O) {
    __shared__ __attribute__((aligned(16))) u16 sK[2][KC * KSTR];
    __shared__ __attribute__((aligned(16))) u16 sV[2][DP * VSTR];
    const int tid = threadIdx.x, lane = tid & 63, wid = tid >> 6;
    const int r32 = lane & 31, hi = lane >> 5;
    const int hh = blockIdx.y;
    const int q0 = blockIdx.x * 128 + wid * 32;
    const u16* Q = qkv + (size_t)hh * SEQ * DP;
    const u16* Kp = qkv + (size_t)NH * SEQ * DP + (size_t)hh * SEQ * DP;
    const u16* Vt = qkv + (size_t)2 * NH * SEQ * DP + (size_t)hh * DP * SEQ;

    bf16x8 qf[2];
#pragma unroll
    for (int s = 0; s < 2; ++s) qf[s] = *reinterpret_cast<const bf16x8*>(Q + (size_t)(q0 + r32) * DP + s * 16 + hi * 8);

    const float C2 = 0.23570226039551584f * 1.4426950408889634f;
    float mrun = -INFINITY;
    f32x16 o = f32x16{};

    bf16x8 rk[2], rv[2];
    auto load_tiles = [&](int k0) {
#pragma unroll
        for (int i = 0; i < 2; ++i) {
            const int idx = tid + i * 256;
            rk[i] = *reinterpret_cast<const bf16x8*>(Kp + (size_t)(k0 + (idx >> 2)) * DP + (idx & 3) * 8);
            rv[i] = *reinterpret_cast<const bf16x8*>(Vt + (size_t)(idx >> 4) * SEQ + k0 + (idx & 15) * 8);
        }
    };
    auto store_tiles = [&](int buf) {
#pragma unroll
        for (int i = 0; i < 2; ++i) {
            const int idx = tid + i * 256;
            *reinterpret_cast<bf16x8*>(&sK[buf][(idx >> 2) * KSTR + (idx & 3) * 8]) = rk[i];
            *reinterpret_cast<bf16x8*>(&sV[buf][(idx >> 4) * VSTR + (idx & 15) * 8]) = rv[i];
        }
    };

    constexpr int NT = SEQ / KC;
    load_tiles(0);
    store_tiles(0);
    __syncthreads();
    for (int t = 0; t < NT; ++t) {
        const int buf = t & 1;
        if (t + 1 < NT) load_tiles((t + 1) * KC);
#pragma unroll
        for (int c = 0; c < KC / 64; ++c) {
            f32x16 s0 = f32x16{}, s1 = f32x16{};
            {
                const u16* kb = &sK[buf][(c * 64 + r32) * KSTR + hi * 8];
                bf16x8 k00 = *reinterpret_cast<const bf16x8*>(kb);
                bf16x8 k01 = *reinterpret_cast<const bf16x8*>(kb + 16);
                bf16x8 k10 = *reinterpret_cast<const bf16x8*>(kb + 32 * KSTR);
                bf16x8 k11 = *reinterpret_cast<const bf16x8*>(kb + 32 * KSTR + 16);
                s0 = __builtin_amdgcn_mfma_f32_32x32x16_bf16(k00, qf[0], s0, 0, 0, 0);
                s1 = __builtin_amdgcn_mfma_f32_32x32x16_bf16(k10, qf[0], s1, 0, 0, 0);
                s0 = __builtin_amdgcn_mfma_f32_32x32x16_bf16(k01, qf[1], s0, 0, 0, 0);
                s1 = __builtin_amdgcn_mfma_f32_32x32x16_bf16(k11, qf[1], s1, 0, 0, 0);
            }
            float tm = fmaxf(s0[0], s1[0]);
#pragma unroll
            for (int r = 1; r < 16; ++r) tm = fmaxf(tm, fmaxf(s0[r], s1[r]));
            tm = fmaxf(tm, __shfl_xor(tm, 32));
            const float mnew = fmaxf(mrun, tm);
            const float alpha = exp2f((mrun - mnew) * C2);
            mrun = mnew;
            const float nm = -mnew * C2;
#pragma unroll
            for (int r = 0; r < 16; ++r) o[r] *= alpha;
#pragma unroll
            for (int r = 0; r < 16; ++r) { s0[r] = exp2f(fmaf(s0[r], C2, nm)); s1[r] = exp2f(fmaf(s1[r], C2, nm)); }
#pragma unroll
            for (int ks = 0; ks < 4; ++ks) {
                bf16x8 pf;
#pragma unroll
                for (int j = 0; j < 8; ++j) {
                    const float pv = (ks < 2) ? s0[(ks & 1) * 8 + j] : s1[(ks & 1) * 8 + j];
                    pf[j] = (short)f2bf(pv);
                }
                const u16* vb = &sV[buf][r32 * VSTR + c * 64 + ks * 16 + 4 * hi];
                s16x4 lo = *reinterpret_cast<const s16x4*>(vb);
                s16x4 hi4 = *reinterpret_cast<const s16x4*>(vb + 8);
                bf16x8 vf = bf16x8{lo[0], lo[1], lo[2], lo[3], hi4[0], hi4[1], hi4[2], hi4[3]};
                o = __builtin_amdgcn_mfma_f32_32x32x16_bf16(vf, pf, o, 0, 0, 0);
            }
        }
        if (t + 1 < NT) store_tiles(buf ^ 1);
        __syncthreads();
    }
    const float l = __shfl(o[10], r32);
    const float inv = 1.f / l;
    u16* orow = O + (size_t)(q0 + r32) * DM + hh * DH;
#pragma unroll
    for (int r = 0; r < 16; ++r) {
        const int d = (r & 3) + 8 * (r >> 2) + 4 * hi;
        if (d < DH) orow[d] = f2bf(o[r] * inv);
    }
}

extern "C" void kernel_launch(void* const* d_in, const int* in_sizes, int n_in,
                              void* d_out, int out_size, void* d_ws, size_t ws_size,
                              hipStream_t stream) {
    const u16* x = (const u16*)d_in[0];
    const u16* wqkv = (const u16*)d_in[1];
    const u16* wo = (const u16*)d_in[2];
    const u16* w1 = (const u16*)d_in[4];
    const u16* w2 = (const u16*)d_in[5];
    const u16* w3 = (const u16*)d_in[6];
    const u16* w4 = (const u16*)d_in[7];
    u16* out = (u16*)d_out;

    u16* ws = (u16*)d_ws;
    u16* qkv = ws;
    u16* Ob = qkv + (size_t)3 * NH * SEQ * DP;
    u16* hb = Ob + (size_t)SEQ * DM;
    u16* yb = hb + (size_t)SEQ * DM;
    u16* hm = yb + (size_t)SEQ * DM;
    u16* x1 = hm + (size_t)SEQ * DM;
    u16* gb = x1 + (size_t)SEQ * DM;

    {
        GemmArgs a{x, wqkv, qkv, nullptr, nullptr, nullptr, 3 * DM, DM};
        gemm_k<128, 96, PRO_LN, EPI_QKV><<<dim3(SEQ / 128, 3 * DM / 96), 256, 0, stream>>>(a);
        pad_qkv_k<<<NH * SEQ / 256, 256, 0, stream>>>(qkv);
    }
    attn_k<<<dim3(SEQ / 128, NH), 256, 0, stream>>>(qkv, Ob);
    {
        GemmArgs a{Ob, wo, hb, x, nullptr, nullptr, DM, DM};
        gemm_k<64, 96, PRO_NONE, EPI_RES><<<dim3(SEQ / 64, DM / 96), 256, 0, stream>>>(a);
    }
    {
        GemmArgs a{hb, w1, hm, nullptr, nullptr, yb, DM, DM};
        gemm_k<64, 96, PRO_LN2, EPI_GELU><<<dim3(SEQ / 64, DM / 96), 256, 0, stream>>>(a);
    }
    {
        GemmArgs a{hm, w2, x1, yb, nullptr, nullptr, DM, DM};
        gemm_k<64, 96, PRO_SHIFT, EPI_RES><<<dim3(SEQ / 64, DM / 96), 256, 0, stream>>>(a);
    }
    {
        GemmArgs a{x1, w3, gb, nullptr, nullptr, nullptr, FF, DM};
        gemm_k<128, 96, PRO_LN, EPI_GELU><<<dim3(SEQ / 128, FF / 96), 256, 0, stream>>>(a);
    }
    {
        GemmArgs a{gb, w4, out, x1, hb, nullptr, DM, FF};
        gemm_k<64, 96, PRO_NONE, EPI_RES2><<<dim3(SEQ / 64, DM / 96), 256, 0, stream>>>(a);
    }
}
```

```cpp
#include <hip/hip_runtime.h>
#include <hip/hip_bf16.h>
#include <stdint.h>

static constexpr int SEQ = 2048, DM = 576, NH = 32, DH = 18, FF = 4608;
static constexpr int DP = 32;

using bf16x8 = __attribute__((ext_vector_type(8))) short;
using s16x4 = __attribute__((ext_vector_type(4))) short;
using f32x4 = __attribute__((ext_vector_type(4))) float;
using f32x16 = __attribute__((ext_vector_type(16))) float;
using u32x4 = __attribute__((ext_vector_type(4))) unsigned;
typedef short v4i16_t __attribute__((ext_vector_type(4)));
typedef unsigned short u16;

__device__ __forceinline__ float bf2f(u16 v) { return __uint_as_float(((unsigned)v) << 16); }
__device__ __forceinline__ u16 f2bf(float f) {
    unsigned u = __float_as_uint(f);
    u += 0x7FFF + ((u >> 16) & 1);
    return (u16)(u >> 16);
}
__device__ __forceinline__ float rbf(float f) { return bf2f(f2bf(f)); }

__device__ __forceinline__ float gelu_tanh(float x) {
    const float u = 0.7978845608028654f * (x + 0.044715f * x * x * x);
    const float e = __expf(2.f * u);
    const float t = 1.f - 2.f / (e + 1.f);
    return 0.5f * x * (1.f + t);
}


struct GemmArgs {
    const u16* A;
    const u16* B;
    u16* C;
    const u16* R1;
    const u16* R2;
    int N;
    int K;
};
enum { EPI_PLAIN = 0, EPI_RES = 1, EPI_GELU = 2, EPI_RES2 = 3 };

template <int BN>
__device__ __forceinline__ int swzB(int row) {
    if constexpr (BN == 64) return ((((row >> 1) & 1) | (((row >> 3) & 1) << 1)) << 1);
    else return (((row & 3) << 1) | (((row >> 3) & 1) << 3));
}

template <int BM, int BN, int NS, int EPI, bool SHIFT>
__global__ __launch_bounds__(256) void gemm_k(GemmArgs a) {
    constexpr int BK = 64;
    constexpr int WM = BM / 2, WN = BN / 2, TM = WM / 16, TN = WN / 16;
    constexpr int ACH = BM * 8 / 256, BCH = BK * BN / 8 / 256, L = ACH + BCH;
    constexpr int ASZ = BM * BK, BSZ = BK * BN, STG = ASZ + BSZ;
    constexpr int CSTR = BN + 8;
    static_assert(BM * CSTR <= NS * STG, "epilogue tile must fit");
    __shared__ __attribute__((aligned(16))) u16 smem[NS * STG];

    const int tid = threadIdx.x, lane = tid & 63, wid = tid >> 6;
    const int wm = wid >> 1, wn = wid & 1;
    const int m0 = blockIdx.x * BM, n0 = blockIdx.y * BN;
    const int K = a.K, N = a.N, nk = K / BK;

    const u16* asrc[ACH];
    const u16* bsrc[BCH];
#pragma unroll
    for (int i = 0; i < ACH; ++i) {
        const int idx = tid + i * 256, row = idx >> 3, ch = (idx & 7) ^ (row & 7);
        asrc[i] = a.A + (size_t)(m0 + row) * K + ch * 8;
    }
#pragma unroll
    for (int i = 0; i < BCH; ++i) {
        const int idx = tid + i * 256, row = idx / (BN / 8), ch = (idx % (BN / 8)) ^ swzB<BN>(row);
        bsrc[i] = a.B + (size_t)row * N + n0 + ch * 8;
    }
    auto issue = [&](int kt) {
        u16* st = smem + (kt % NS) * STG;
#pragma unroll
        for (int i = 0; i < ACH; ++i) {
            long off = (long)kt * BK;
            if constexpr (SHIFT) {
                const int col = kt * BK + (((tid + i * 256) & 7) ^ (((tid + i * 256) >> 3) & 7)) * 8;
                const int grp = col / 144;
                off += (long)K * ((grp == 0) ? -1 : (grp == 1) ? 1 : (grp == 2) ? -2 : 2);
            }
            __builtin_amdgcn_global_load_lds((const unsigned*)(asrc[i] + off), (unsigned*)(st + (tid + i * 256) * 8), 16, 0, 0);
        }
#pragma unroll
        for (int i = 0; i < BCH; ++i)
            __builtin_amdgcn_global_load_lds((const unsigned*)(bsrc[i] + (size_t)kt * BK * N), (unsigned*)(st + ASZ + (tid + i * 256) * 8), 16, 0, 0);
    };

    f32x4 acc[TM][TN];
#pragma unroll
    for (int i = 0; i < TM; ++i)
#pragma unroll
        for (int j = 0; j < TN; ++j) acc[i][j] = f32x4{0.f, 0.f, 0.f, 0.f};

#pragma unroll
    for (int s = 0; s < NS - 1; ++s) if (s < nk) issue(s);

    for (int kt = 0; kt < nk; ++kt) {
        if (kt + NS - 2 < nk) asm volatile("s_waitcnt vmcnt(%0) lgkmcnt(0)" :: "i"((NS - 2) * L) : "memory");
        else asm volatile("s_waitcnt vmcnt(0) lgkmcnt(0)" ::: "memory");
        __builtin_amdgcn_s_barrier();
        asm volatile("" ::: "memory");
        if (kt + NS - 1 < nk) issue(kt + NS - 1);
        const u16* sA = smem + (kt % NS) * STG;
        const u16* sB = sA + ASZ;
#pragma unroll
        for (int kk = 0; kk < 2; ++kk) {
            bf16x8 af[TM], bfr[TN];
#pragma unroll
            for (int i = 0; i < TM; ++i) {
                const int row = wm * WM + i * 16 + (lane & 15);
                af[i] = *reinterpret_cast<const bf16x8*>(sA + row * BK + ((kk * 4 + (lane >> 4)) ^ (row & 7)) * 8);
            }
#pragma unroll
            for (int j = 0; j < TN; ++j) {
                const int g = lane >> 4, q = (lane & 15) >> 2, p = lane & 3;
                const int r0 = kk * 32 + 8 * g + q, r1 = r0 + 4;
                const int ch = (wn * WN + j * 16) / 8 + (p >> 1);
                const u16* p0 = sB + r0 * BN + (ch ^ swzB<BN>(r0)) * 8 + (p & 1) * 4;
                const u16* p1 = sB + r1 * BN + (ch ^ swzB<BN>(r1)) * 8 + (p & 1) * 4;
                s16x4 lo = __builtin_bit_cast(s16x4, __builtin_amdgcn_ds_read_tr16_b64_v4i16((__attribute__((address_space(3))) v4i16_t*)p0));
                s16x4 hi = __builtin_bit_cast(s16x4, __builtin_amdgcn_ds_read_tr16_b64_v4i16((__attribute__((address_space(3))) v4i16_t*)p1));
                bfr[j] = bf16x8{lo[0], lo[1], lo[2], lo[3], hi[0], hi[1], hi[2], hi[3]};
            }
#pragma unroll
            for (int i = 0; i < TM; ++i)
#pragma unroll
                for (int j = 0; j < TN; ++j)
                    acc[i][j] = __builtin_amdgcn_mfma_f32_16x16x32_bf16(af[i], bfr[j], acc[i][j], 0, 0, 0);
        }
    }
    __syncthreads();
#pragma unroll
    for (int i = 0; i < TM; ++i)
#pragma unroll
        for (int j = 0; j < TN; ++j)
#pragma unroll
            for (int r = 0; r < 4; ++r)
                smem[(wm * WM + i * 16 + (lane >> 4) * 4 + r) * CSTR + wn * WN + j * 16 + (lane & 15)] = f2bf(acc[i][j][r]);
    __syncthreads();
    constexpr int CCH = BM * BN / 8 / 256;
#pragma unroll
    for (int i = 0; i < CCH; ++i) {
        const int idx = tid + i * 256, row = idx / (BN / 8), ch = idx % (BN / 8);
        bf16x8 v = *reinterpret_cast<const bf16x8*>(smem + row * CSTR + ch * 8);
        const size_t go = (size_t)(m0 + row) * N + n0 + ch * 8;
        if constexpr (EPI == EPI_RES || EPI == EPI_RES2) {
            bf16x8 r1 = *reinterpret_cast<const bf16x8*>(a.R1 + go);
#pragma unroll
            for (int j = 0; j < 8; ++j) v[j] = (short)f2bf(bf2f((u16)r1[j]) + bf2f((u16)v[j]));
            if constexpr (EPI == EPI_RES2) {
                bf16x8 r2 = *reinterpret_cast<const bf16x8*>(a.R2 + go);
#pragma unroll
                for (int j = 0; j < 8; ++j) v[j] = (short)f2bf(bf2f((u16)r2[j]) + bf2f((u16)v[j]));
            }
        } else if constexpr (EPI == EPI_GELU) {
#pragma unroll
            for (int j = 0; j < 8; ++j) v[j] = (short)f2bf(gelu_tanh(bf2f((u16)v[j])));
        }
        *reinterpret_cast<bf16x8*>(a.C + go) = v;
    }
}

__device__ __forceinline__ float wsum(float v) {
#pragma unroll
    for (int o = 32; o > 0; o >>= 1) v += __shfl_xor(v, o);
    return v;
}
__device__ __forceinline__ void ln_regs(float (&v)[16], int cnt) {
    float s = 0.f;
#pragma unroll
    for (int j = 0; j < 16; ++j) s += (j < cnt) ? v[j] : 0.f;
    const float mean = wsum(s) * (1.f / DM);
    float q = 0.f;
#pragma unroll
    for (int j = 0; j < 16; ++j) { const float d = v[j] - mean; q += (j < cnt) ? d * d : 0.f; }
    const float var = wsum(q) * (1.f / DM);
    const float mb = rbf(mean), sd = rbf(sqrtf(rbf(rbf(var) + 1e-5f)));
#pragma unroll
    for (int j = 0; j < 16; ++j) v[j] = rbf(rbf(v[j] - mb) / sd);
}
template <bool DOUBLE>
__global__ __launch_bounds__(256) void ln_k(const u16* __restrict__ X, u16* __restrict__ Y, u16* __restrict__ Z, u16* __restrict__ pad) {
    const int lane = threadIdx.x & 63, row = blockIdx.x * 4 + (threadIdx.x >> 6);
    const int cnt = lane < 8 ? 16 : 8;
    float v[16];
    bf16x8 t0 = *reinterpret_cast<const bf16x8*>(X + (size_t)row * DM + lane * 8);
    bf16x8 t1 = t0;
    if (lane < 8) t1 = *reinterpret_cast<const bf16x8*>(X + (size_t)row * DM + 512 + lane * 8);
#pragma unroll
    for (int j = 0; j < 8; ++j) { v[j] = bf2f((u16)t0[j]); v[8 + j] = bf2f((u16)t1[j]); }
    ln_regs(v, cnt);
#pragma unroll
    for (int j = 0; j < 8; ++j) { t0[j] = (short)f2bf(v[j]); t1[j] = (short)f2bf(v[8 + j]); }
    *reinterpret_cast<bf16x8*>(Y + (size_t)row * DM + lane * 8) = t0;
    if (lane < 8) *reinterpret_cast<bf16x8*>(Y + (size_t)row * DM + 512 + lane * 8) = t1;
    if constexpr (DOUBLE) {
        ln_regs(v, cnt);
#pragma unroll
        for (int j = 0; j < 8; ++j) { t0[j] = (short)f2bf(v[j]); t1[j] = (short)f2bf(v[8 + j]); }
        *reinterpret_cast<bf16x8*>(Z + (size_t)row * DM + lane * 8) = t0;
        if (lane < 8) *reinterpret_cast<bf16x8*>(Z + (size_t)row * DM + 512 + lane * 8) = t1;
        if (blockIdx.x == 0) {
            for (int i = threadIdx.x; i < 2 * DM; i += 256) { pad[i] = 0; pad[(size_t)(SEQ + 2) * DM + i] = 0; }
        }
    }
}

static constexpr int KC = 128;
static constexpr int KSTR = 40;
static constexpr int QKVS = 3 * DM;

__global__ __launch_bounds__(256) void attn_k(const u16* __restrict__ qkv, u16* __restrict__ O) {
    __shared__ __attribute__((aligned(16))) u16 sK[2][KC * KSTR];
    __shared__ __attribute__((aligned(16))) u16 sV[2][KC * KSTR];
    const int tid = threadIdx.x, lane = tid & 63, wid = tid >> 6;
    const int r32 = lane & 31, hi = lane >> 5;
    const int hh = blockIdx.y;
    const int q0 = blockIdx.x * 128 + wid * 32;

    for (int i = tid; i < 2 * KC * 7; i += 256) {
        const int b = i / (KC * 7), rem = i % (KC * 7), r = rem / 7, c = 18 + (rem % 7) * 2;
        *reinterpret_cast<unsigned*>(&sK[b][r * KSTR + c]) = 0u;
        *reinterpret_cast<unsigned*>(&sV[b][r * KSTR + c]) = (c == 18) ? 0x3F80u : 0u;
    }
    bf16x8 qf[2];
    {
        const unsigned* qp = reinterpret_cast<const unsigned*>(qkv + (size_t)(q0 + r32) * QKVS + hh * DH);
        u32x4 w0 = hi ? u32x4{qp[4], qp[5], qp[6], qp[7]} : u32x4{qp[0], qp[1], qp[2], qp[3]};
        u32x4 w1 = hi ? u32x4{0u, 0u, 0u, 0u} : u32x4{qp[8], 0u, 0u, 0u};
        qf[0] = __builtin_bit_cast(bf16x8, w0);
        qf[1] = __builtin_bit_cast(bf16x8, w1);
    }
    const float C2 = 0.23570226039551584f * 1.4426950408889634f;
    float mrun = -INFINITY;
    f32x16 o = f32x16{};

    unsigned rk[5], rv[5];
    auto load_tiles = [&](int k0) {
#pragma unroll
        for (int i = 0; i < 5; ++i) {
            const int idx = tid + i * 256;
            if (idx < KC * 9) {
                const int r = idx / 9, c = idx % 9;
                const unsigned* p = reinterpret_cast<const unsigned*>(qkv + (size_t)(k0 + r) * QKVS + DM + hh * DH) + c;
                rk[i] = p[0];
                rv[i] = p[DM / 2];
            }
        }
    };
    auto store_tiles = [&](int buf) {
#pragma unroll
        for (int i = 0; i < 5; ++i) {
            const int idx = tid + i * 256;
            if (idx < KC * 9) {
                const int r = idx / 9, c = idx % 9;
                *reinterpret_cast<unsigned*>(&sK[buf][r * KSTR + c * 2]) = rk[i];
                *reinterpret_cast<unsigned*>(&sV[buf][r * KSTR + c * 2]) = rv[i];
            }
        }
    };

    constexpr int NT = SEQ / KC;
    load_tiles(0);
    store_tiles(0);
    __syncthreads();
    for (int t = 0; t < NT; ++t) {
        const int buf = t & 1;
        if (t + 1 < NT) load_tiles((t + 1) * KC);
#pragma unroll
        for (int c = 0; c < KC / 64; ++c) {
            f32x16 s0 = f32x16{}, s1 = f32x16{};
            {
                const u16* kb = &sK[buf][(c * 64 + r32) * KSTR + hi * 8];
                bf16x8 k00 = *reinterpret_cast<const bf16x8*>(kb);
                bf16x8 k01 = *reinterpret_cast<const bf16x8*>(kb + 16);
                bf16x8 k10 = *reinterpret_cast<const bf16x8*>(kb + 32 * KSTR);
                bf16x8 k11 = *reinterpret_cast<const bf16x8*>(kb + 32 * KSTR + 16);
                s0 = __builtin_amdgcn_mfma_f32_32x32x16_bf16(k00, qf[0], s0, 0, 0, 0);
                s1 = __builtin_amdgcn_mfma_f32_32x32x16_bf16(k10, qf[0], s1, 0, 0, 0);
                s0 = __builtin_amdgcn_mfma_f32_32x32x16_bf16(k01, qf[1], s0, 0, 0, 0);
                s1 = __builtin_amdgcn_mfma_f32_32x32x16_bf16(k11, qf[1], s1, 0, 0, 0);
            }
            float tm = fmaxf(s0[0], s1[0]);
#pragma unroll
            for (int r = 1; r < 16; ++r) tm = fmaxf(tm, fmaxf(s0[r], s1[r]));
            tm = fmaxf(tm, __shfl_xor(tm, 32));
            if (!__all(tm <= mrun)) {
                const float mnew = fmaxf(mrun, tm);
                const float alpha = __builtin_amdgcn_exp2f((mrun - mnew) * C2);
                mrun = mnew;
#pragma unroll
                for (int r = 0; r < 16; ++r) o[r] *= alpha;
            }
            const float nm = -mrun * C2;
#pragma unroll
            for (int r = 0; r < 16; ++r) {
                s0[r] = __builtin_amdgcn_exp2f(fmaf(s0[r], C2, nm));
                s1[r] = __builtin_amdgcn_exp2f(fmaf(s1[r], C2, nm));
            }
#pragma unroll
            for (int ks = 0; ks < 4; ++ks) {
                u32x4 pw;
#pragma unroll
                for (int j = 0; j < 4; ++j) {
                    const float a0 = (ks < 2) ? s0[(ks & 1) * 8 + 2 * j] : s1[(ks & 1) * 8 + 2 * j];
                    const float a1 = (ks < 2) ? s0[(ks & 1) * 8 + 2 * j + 1] : s1[(ks & 1) * 8 + 2 * j + 1];
                    unsigned r;
                    asm("v_cvt_pk_bf16_f32 %0, %1, %2" : "=v"(r) : "v"(a0), "v"(a1));
                    pw[j] = r;
                }
                const bf16x8 pf = __builtin_bit_cast(bf16x8, pw);
                const int q = (lane & 15) >> 2, p = lane & 3, cb = ((lane >> 4) & 1) * 16 + 4 * p;
                const u16* vb = &sV[buf][(c * 64 + ks * 16 + 4 * hi + q) * KSTR + cb];
                s16x4 lo = __builtin_bit_cast(s16x4, __builtin_amdgcn_ds_read_tr16_b64_v4i16((__attribute__((address_space(3))) v4i16_t*)vb));
                s16x4 hi4 = __builtin_bit_cast(s16x4, __builtin_amdgcn_ds_read_tr16_b64_v4i16((__attribute__((address_space(3))) v4i16_t*)(vb + 8 * KSTR)));
                bf16x8 vf = bf16x8{lo[0], lo[1], lo[2], lo[3], hi4[0], hi4[1], hi4[2], hi4[3]};
                o = __builtin_amdgcn_mfma_f32_32x32x16_bf16(vf, pf, o, 0, 0, 0);
            }
        }
        if (t + 1 < NT) store_tiles(buf ^ 1);
        __syncthreads();
    }
    const float l = __shfl(o[10], r32);
    const float inv = 1.f / l;
    u16* orow = O + (size_t)(q0 + r32) * DM + hh * DH;
#pragma unroll
    for (int r = 0; r < 16; ++r) {
        const int d = (r & 3) + 8 * (r >> 2) + 4 * hi;
        if (d < DH) orow[d] = f2bf(o[r] * inv);
    }
}

extern "C" void kernel_launch(void* const* d_in, const int* in_sizes, int n_in,
                              void* d_out, int out_size, void* d_ws, size_t ws_size,
                              hipStream_t stream) {
    const u16* x = (const u16*)d_in[0];
    const u16* wqkv = (const u16*)d_in[1];
    const u16* wo = (const u16*)d_in[2];
    const u16* w1 = (const u16*)d_in[4];
    const u16* w2 = (const u16*)d_in[5];
    const u16* w3 = (const u16*)d_in[6];
    const u16* w4 = (const u16*)d_in[7];
    u16* out = (u16*)d_out;

    const size_t T = (size_t)SEQ * DM;
    u16* ws = (u16*)d_ws;
    u16* qkv = ws;
    u16* xn = qkv + 3 * T;
    u16* Ob = xn + T;
    u16* hb = Ob + T;
    u16* yb = hb + T;
    u16* zb = yb + T;
    u16* hmp = zb + T;
    u16* hm = hmp + 2 * DM;
    u16* x1 = hmp + T + 4 * DM;
    u16* x1n = x1 + T;
    u16* gb = x1n + T;

    ln_k<false><<<SEQ / 4, 256, 0, stream>>>(x, xn, nullptr, nullptr);
    gemm_k<128, 64, 4, EPI_PLAIN, false><<<dim3(SEQ / 128, 3 * DM / 64), 256, 0, stream>>>(GemmArgs{xn, wqkv, qkv, nullptr, nullptr, 3 * DM, DM});
    attn_k<<<dim3(SEQ / 128, NH), 256, 0, stream>>>(qkv, Ob);
    gemm_k<64, 64, 4, EPI_RES, false><<<dim3(SEQ / 64, DM / 64), 256, 0, stream>>>(GemmArgs{Ob, wo, hb, x, nullptr, DM, DM});
    ln_k<true><<<SEQ / 4, 256, 0, stream>>>(hb, yb, zb, hmp);
    gemm_k<64, 64, 4, EPI_GELU, false><<<dim3(SEQ / 64, DM / 64), 256, 0, stream>>>(GemmArgs{zb, w1, hm, nullptr, nullptr, DM, DM});
    gemm_k<64, 64, 4, EPI_RES, true><<<dim3(SEQ / 64, DM / 64), 256, 0, stream>>>(GemmArgs{hm, w2, x1, yb, nullptr, DM, DM});
    ln_k<false><<<SEQ / 4, 256, 0, stream>>>(x1, x1n, nullptr, nullptr);
    gemm_k<128, 128, 3, EPI_GELU, false><<<dim3(SEQ / 128, FF / 128), 256, 0, stream>>>(GemmArgs{x1n, w3, gb, nullptr, nullptr, FF, DM});
    gemm_k<64, 64, 4, EPI_RES2, false><<<dim3(SEQ / 64, DM / 64), 256, 0, stream>>>(GemmArgs{gb, w4, out, x1, hb, DM, FF});
}
```

```cpp
#include <hip/hip_runtime.h>
#include <hip/hip_bf16.h>
#include <stdint.h>

static constexpr int SEQ = 2048, DM = 576, NH = 32, DH = 18, FF = 4608;
static constexpr int DP = 32;

using bf16x8 = __attribute__((ext_vector_type(8))) short;
using s16x4 = __attribute__((ext_vector_type(4))) short;
using f32x4 = __attribute__((ext_vector_type(4))) float;
using f32x16 = __attribute__((ext_vector_type(16))) float;
using u32x4 = __attribute__((ext_vector_type(4))) unsigned;
typedef short v4i16_t __attribute__((ext_vector_type(4)));
typedef unsigned short u16;

__device__ __forceinline__ float bf2f(u16 v) { return __uint_as_float(((unsigned)v) << 16); }
__device__ __forceinline__ u16 f2bf(float f) {
    unsigned u = __float_as_uint(f);
    u += 0x7FFF + ((u >> 16) & 1);
    return (u16)(u >> 16);
}
__device__ __forceinline__ float rbf(float f) { return bf2f(f2bf(f)); }

__device__ __forceinline__ float gelu_tanh(float x) {
    const float u = 0.7978845608028654f * (x + 0.044715f * x * x * x);
    const float e = __expf(2.f * u);
    const float t = 1.f - 2.f / (e + 1.f);
    return 0.5f * x * (1.f + t);
}


struct GemmArgs {
    const u16* A;
    const u16* B;
    u16* C;
    const u16* R1;
    const u16* R2;
    int N;
    int K;
};
enum { EPI_PLAIN = 0, EPI_RES = 1, EPI_GELU = 2, EPI_RES2 = 3 };

template <int BN>
__device__ __forceinline__ int swzB(int row) {
    if constexpr (BN == 64) return ((((row >> 1) & 1) | (((row >> 3) & 1) << 1)) << 1);
    else return (((row & 3) << 1) | (((row >> 3) & 1) << 3));
}

template <int BM, int BN, int NS, int EPI, bool SHIFT>
__global__ __launch_bounds__(256) void gemm_k(GemmArgs a) {
    constexpr int BK = 64;
    constexpr int WM = BM / 2, WN = BN / 2, TM = WM / 16, TN = WN / 16;
    constexpr int ACH = BM * 8 / 256, BCH = BK * BN / 8 / 256, L = ACH + BCH;
    constexpr int ASZ = BM * BK, BSZ = BK * BN, STG = ASZ + BSZ;
    constexpr int CSTR = BN + 8;
    static_assert(BM * CSTR <= NS * STG, "epilogue tile must fit");
    __shared__ __attribute__((aligned(16))) u16 smem[NS * STG];

    const int tid = threadIdx.x, lane = tid & 63, wid = tid >> 6;
    const int wm = wid >> 1, wn = wid & 1;
    const int lb = blockIdx.y * gridDim.x + blockIdx.x, jj = lb >> 3;
    const int m0 = ((lb & 7) + 8 * (jj / (int)gridDim.y)) * BM, n0 = (jj % (int)gridDim.y) * BN;
    const int K = a.K, N = a.N, nk = K / BK;

    const u16* asrc[ACH];
    const u16* bsrc[BCH];
#pragma unroll
    for (int i = 0; i < ACH; ++i) {
        const int idx = tid + i * 256, row = idx >> 3, ch = (idx & 7) ^ (row & 7);
        asrc[i] = a.A + (size_t)(m0 + row) * K + ch * 8;
    }
#pragma unroll
    for (int i = 0; i < BCH; ++i) {
        const int idx = tid + i * 256, row = idx / (BN / 8), ch = (idx % (BN / 8)) ^ swzB<BN>(row);
        bsrc[i] = a.B + (size_t)row * N + n0 + ch * 8;
    }
    auto issue = [&](int kt, int slot) {
        u16* st = smem + slot * STG;
#pragma unroll
        for (int i = 0; i < ACH; ++i) {
            long off = (long)kt * BK;
            if constexpr (SHIFT) {
                const int col = kt * BK + (((tid + i * 256) & 7) ^ (((tid + i * 256) >> 3) & 7)) * 8;
                const int grp = col / 144;
                off += (long)K * ((grp == 0) ? -1 : (grp == 1) ? 1 : (grp == 2) ? -2 : 2);
            }
            __builtin_amdgcn_global_load_lds((const unsigned*)(asrc[i] + off), (unsigned*)(st + (tid + i * 256) * 8), 16, 0, 0);
        }
#pragma unroll
        for (int i = 0; i < BCH; ++i)
            __builtin_amdgcn_global_load_lds((const unsigned*)(bsrc[i] + (size_t)kt * BK * N), (unsigned*)(st + ASZ + (tid + i * 256) * 8), 16, 0, 0);
    };

    f32x4 acc[TM][TN];
#pragma unroll
    for (int i = 0; i < TM; ++i)
#pragma unroll
        for (int j = 0; j < TN; ++j) acc[i][j] = f32x4{0.f, 0.f, 0.f, 0.f};

#pragma unroll
    for (int s = 0; s < NS - 1; ++s) if (s < nk) issue(s, s);

    for (int kt0 = 0; kt0 < nk; kt0 += NS)
#pragma unroll
    for (int sl = 0; sl < NS; ++sl) {
        const int kt = kt0 + sl;
        if (kt + NS - 2 < nk) asm volatile("s_waitcnt vmcnt(%0) lgkmcnt(0)" :: "i"((NS - 2) * L) : "memory");
        else asm volatile("s_waitcnt vmcnt(0) lgkmcnt(0)" ::: "memory");
        __builtin_amdgcn_s_barrier();
        asm volatile("" ::: "memory");
        if (kt + NS - 1 < nk) issue(kt + NS - 1, (sl + NS - 1) % NS);
        const u16* sA = smem + sl * STG;
        const u16* sB = sA + ASZ;
#pragma unroll
        for (int kk = 0; kk < 2; ++kk) {
            bf16x8 af[TM], bfr[TN];
            s16x4 blo[TN], bhi[TN];
#pragma unroll
            for (int i = 0; i < TM; ++i) {
                const int row = wm * WM + i * 16 + (lane & 15);
                af[i] = *reinterpret_cast<const bf16x8*>(sA + row * BK + ((kk * 4 + (lane >> 4)) ^ (row & 7)) * 8);
            }
#pragma unroll
            for (int j = 0; j < TN; ++j) {
                const int g = lane >> 4, q = (lane & 15) >> 2, p = lane & 3;
                const int r0 = kk * 32 + 8 * g + q, r1 = r0 + 4;
                const int ch = (wn * WN + j * 16) / 8 + (p >> 1);
                const u16* p0 = sB + r0 * BN + (ch ^ swzB<BN>(r0)) * 8 + (p & 1) * 4;
                const u16* p1 = sB + r1 * BN + (ch ^ swzB<BN>(r1)) * 8 + (p & 1) * 4;
                s16x4 lo, hi;
                asm volatile("ds_read_b64_tr_b16 %0, %1" : "=v"(lo) : "v"((unsigned)(uintptr_t)p0) : "memory");
                asm volatile("ds_read_b64_tr_b16 %0, %1" : "=v"(hi) : "v"((unsigned)(uintptr_t)p1) : "memory");
                blo[j] = lo; bhi[j] = hi;
            }
#pragma unroll
            for (int j = 0; j < TN; ++j) asm volatile("s_waitcnt lgkmcnt(0)" : "+v"(blo[j]), "+v"(bhi[j]) :: "memory");
#pragma unroll
            for (int j = 0; j < TN; ++j) {
                s16x4 lo = blo[j], hi = bhi[j];
                bfr[j] = bf16x8{lo[0], lo[1], lo[2], lo[3], hi[0], hi[1], hi[2], hi[3]};
            }
#pragma unroll
            for (int i = 0; i < TM; ++i)
#pragma unroll
                for (int j = 0; j < TN; ++j)
                    acc[i][j] = __builtin_amdgcn_mfma_f32_16x16x32_bf16(af[i], bfr[j], acc[i][j], 0, 0, 0);
        }
    }
    __syncthreads();
#pragma unroll
    for (int i = 0; i < TM; ++i)
#pragma unroll
        for (int j = 0; j < TN; ++j)
#pragma unroll
            for (int r = 0; r < 4; ++r)
                smem[(wm * WM + i * 16 + (lane >> 4) * 4 + r) * CSTR + wn * WN + j * 16 + (lane & 15)] = f2bf(acc[i][j][r]);
    __syncthreads();
    constexpr int CCH = BM * BN / 8 / 256;
#pragma unroll
    for (int i = 0; i < CCH; ++i) {
        const int idx = tid + i * 256, row = idx / (BN / 8), ch = idx % (BN / 8);
        bf16x8 v = *reinterpret_cast<const bf16x8*>(smem + row * CSTR + ch * 8);
        const size_t go = (size_t)(m0 + row) * N + n0 + ch * 8;
        if constexpr (EPI == EPI_RES || EPI == EPI_RES2) {
            bf16x8 r1 = *reinterpret_cast<const bf16x8*>(a.R1 + go);
#pragma unroll
            for (int j = 0; j < 8; ++j) v[j] = (short)f2bf(bf2f((u16)r1[j]) + bf2f((u16)v[j]));
            if constexpr (EPI == EPI_RES2) {
                bf16x8 r2 = *reinterpret_cast<const bf16x8*>(a.R2 + go);
#pragma unroll
                for (int j = 0; j < 8; ++j) v[j] = (short)f2bf(bf2f((u16)r2[j]) + bf2f((u16)v[j]));
            }
        } else if constexpr (EPI == EPI_GELU) {
#pragma unroll
            for (int j = 0; j < 8; ++j) v[j] = (short)f2bf(gelu_tanh(bf2f((u16)v[j])));
        }
        *reinterpret_cast<bf16x8*>(a.C + go) = v;
    }
}

__device__ __forceinline__ float wsum(float v) {
#pragma unroll
    for (int o = 32; o > 0; o >>= 1) v += __shfl_xor(v, o);
    return v;
}
__device__ __forceinline__ void ln_regs(float (&v)[16], int cnt) {
    float s = 0.f;
#pragma unroll
    for (int j = 0; j < 16; ++j) s += (j < cnt) ? v[j] : 0.f;
    const float mean = wsum(s) * (1.f / DM);
    float q = 0.f;
#pragma unroll
    for (int j = 0; j < 16; ++j) { const float d = v[j] - mean; q += (j < cnt) ? d * d : 0.f; }
    const float var = wsum(q) * (1.f / DM);
    const float mb = rbf(mean), sd = rbf(sqrtf(rbf(rbf(var) + 1e-5f)));
#pragma unroll
    for (int j = 0; j < 16; ++j) v[j] = rbf(rbf(v[j] - mb) / sd);
}
template <bool DOUBLE>
__global__ __launch_bounds__(256) void ln_k(const u16* __restrict__ X, u16* __restrict__ Y, u16* __restrict__ Z, u16* __restrict__ pad) {
    const int lane = threadIdx.x & 63, row = blockIdx.x * 4 + (threadIdx.x >> 6);
    const int cnt = lane < 8 ? 16 : 8;
    float v[16];
    bf16x8 t0 = *reinterpret_cast<const bf16x8*>(X + (size_t)row * DM + lane * 8);
    bf16x8 t1 = t0;
    if (lane < 8) t1 = *reinterpret_cast<const bf16x8*>(X + (size_t)row * DM + 512 + lane * 8);
#pragma unroll
    for (int j = 0; j < 8; ++j) { v[j] = bf2f((u16)t0[j]); v[8 + j] = bf2f((u16)t1[j]); }
    ln_regs(v, cnt);
#pragma unroll
    for (int j = 0; j < 8; ++j) { t0[j] = (short)f2bf(v[j]); t1[j] = (short)f2bf(v[8 + j]); }
    *reinterpret_cast<bf16x8*>(Y + (size_t)row * DM + lane * 8) = t0;
    if (lane < 8) *reinterpret_cast<bf16x8*>(Y + (size_t)row * DM + 512 + lane * 8) = t1;
    if constexpr (DOUBLE) {
        ln_regs(v, cnt);
#pragma unroll
        for (int j = 0; j < 8; ++j) { t0[j] = (short)f2bf(v[j]); t1[j] = (short)f2bf(v[8 + j]); }
        *reinterpret_cast<bf16x8*>(Z + (size_t)row * DM + lane * 8) = t0;
        if (lane < 8) *reinterpret_cast<bf16x8*>(Z + (size_t)row * DM + 512 + lane * 8) = t1;
        if (blockIdx.x == 0) {
            for (int i = threadIdx.x; i < 2 * DM; i += 256) { pad[i] = 0; pad[(size_t)(SEQ + 2) * DM + i] = 0; }
        }
    }
}

static constexpr int KC = 128;
static constexpr int KSTR = 40;
static constexpr int QKVS = 3 * DM;

__device__ __forceinline__ float max3f(float a, float b, float c) { return __builtin_fmaxf(__builtin_fmaxf(a, b), c); }
__global__ __launch_bounds__(512, 4) void attn_k(const u16* __restrict__ qkv, u16* __restrict__ O) {
    __shared__ __attribute__((aligned(16))) u16 sKV[2][2][2][KC * KSTR];
    const int grp = threadIdx.x >> 8;
    u16 (*sK)[KC * KSTR] = sKV[grp][0];
    u16 (*sV)[KC * KSTR] = sKV[grp][1];
    const int tid = threadIdx.x & 255, lane = tid & 63, wid = tid >> 6;
    const int r32 = lane & 31, hi = lane >> 5;
    const int hh = blockIdx.y;
    const int q0 = blockIdx.x * 128 + wid * 32;

    for (int i = tid; i < 2 * KC * 7; i += 256) {
        const int b = i / (KC * 7), rem = i % (KC * 7), r = rem / 7, c = 18 + (rem % 7) * 2;
        *reinterpret_cast<unsigned*>(&sK[b][r * KSTR + c]) = 0u;
        *reinterpret_cast<unsigned*>(&sV[b][r * KSTR + c]) = (c == 18) ? 0x3F80u : 0u;
    }
    bf16x8 qf[2];
    {
        const unsigned* qp = reinterpret_cast<const unsigned*>(qkv + (size_t)(q0 + r32) * QKVS + hh * DH);
        u32x4 w0 = hi ? u32x4{qp[4], qp[5], qp[6], qp[7]} : u32x4{qp[0], qp[1], qp[2], qp[3]};
        u32x4 w1 = hi ? u32x4{0u, 0u, 0u, 0u} : u32x4{qp[8], 0u, 0u, 0u};
        qf[0] = __builtin_bit_cast(bf16x8, w0);
        qf[1] = __builtin_bit_cast(bf16x8, w1);
    }
    const float C2 = 0.23570226039551584f * 1.4426950408889634f;
    float mrun = -INFINITY;
    f32x16 o = f32x16{};

    unsigned rkA[5], rvA[5], rkB[5], rvB[5];
    auto load_tiles = [&](int k0, unsigned (&rk)[5], unsigned (&rv)[5]) {
#pragma unroll
        for (int i = 0; i < 5; ++i) {
            const int idx = tid + i * 256;
            if (idx < KC * 9) {
                const int r = idx / 9, c = idx % 9;
                const unsigned* p = reinterpret_cast<const unsigned*>(qkv + (size_t)(k0 + r) * QKVS + DM + hh * DH) + c;
                rk[i] = p[0];
                rv[i] = p[DM / 2];
            }
        }
    };
    auto store_tiles = [&](int buf, unsigned (&rk)[5], unsigned (&rv)[5]) {
#pragma unroll
        for (int i = 0; i < 5; ++i) {
            const int idx = tid + i * 256;
            if (idx < KC * 9) {
                const int r = idx / 9, c = idx % 9;
                *reinterpret_cast<unsigned*>(&sK[buf][r * KSTR + c * 2]) = rk[i];
                *reinterpret_cast<unsigned*>(&sV[buf][r * KSTR + c * 2]) = rv[i];
            }
        }
    };

    constexpr int NT = SEQ / KC / 2;
    load_tiles(grp * KC, rkA, rvA);
    load_tiles((2 + grp) * KC, rkB, rvB);
    store_tiles(0, rkA, rvA);
    __syncthreads();
#pragma unroll 2
    for (int t = 0; t < NT; ++t) {
        const int buf = t & 1;
        if (t + 2 < NT) { if (buf == 0) load_tiles((2 * (t + 2) + grp) * KC, rkA, rvA); else load_tiles((2 * (t + 2) + grp) * KC, rkB, rvB); }
#pragma unroll
        for (int c = 0; c < KC / 64; ++c) {
            f32x16 s0 = f32x16{}, s1 = f32x16{};
            {
                const u16* kb = &sK[buf][(c * 64 + r32) * KSTR + hi * 8];
                bf16x8 k00 = *reinterpret_cast<const bf16x8*>(kb);
                bf16x8 k01 = *reinterpret_cast<const bf16x8*>(kb + 16);
                bf16x8 k10 = *reinterpret_cast<const bf16x8*>(kb + 32 * KSTR);
                bf16x8 k11 = *reinterpret_cast<const bf16x8*>(kb + 32 * KSTR + 16);
                s0 = __builtin_amdgcn_mfma_f32_32x32x16_bf16(k00, qf[0], s0, 0, 0, 0);
                s1 = __builtin_amdgcn_mfma_f32_32x32x16_bf16(k10, qf[0], s1, 0, 0, 0);
                s0 = __builtin_amdgcn_mfma_f32_32x32x16_bf16(k01, qf[1], s0, 0, 0, 0);
                s1 = __builtin_amdgcn_mfma_f32_32x32x16_bf16(k11, qf[1], s1, 0, 0, 0);
            }
            float tma = max3f(s0[0], s1[0], s0[1]), tmb = max3f(s1[1], s0[2], s1[2]);
#pragma unroll
            for (int r = 3; r < 15; r += 2) { tma = max3f(tma, s0[r], s1[r]); tmb = max3f(tmb, s0[r + 1], s1[r + 1]); }
            float tm = max3f(tma, tmb, max3f(s0[15], s1[15], mrun));
            {
                auto rr = __builtin_amdgcn_permlane32_swap(__float_as_uint(tm), __float_as_uint(tm), false, false);
                tm = max3f(__uint_as_float(rr[0]), __uint_as_float(rr[1]), mrun);
            }
            if (!__all(tm <= mrun)) {
                const float mnew = tm;
                const float alpha = __builtin_amdgcn_exp2f((mrun - mnew) * C2);
                mrun = mnew;
#pragma unroll
                for (int r = 0; r < 16; ++r) o[r] *= alpha;
            }
            const float nm = -mrun * C2;
#pragma unroll
            for (int r = 0; r < 16; ++r) {
                s0[r] = __builtin_amdgcn_exp2f(fmaf(s0[r], C2, nm));
                s1[r] = __builtin_amdgcn_exp2f(fmaf(s1[r], C2, nm));
            }
#pragma unroll
            for (int ks = 0; ks < 4; ++ks) {
                u32x4 pw;
#pragma unroll
                for (int j = 0; j < 4; ++j) {
                    const float a0 = (ks < 2) ? s0[(ks & 1) * 8 + 2 * j] : s1[(ks & 1) * 8 + 2 * j];
                    const float a1 = (ks < 2) ? s0[(ks & 1) * 8 + 2 * j + 1] : s1[(ks & 1) * 8 + 2 * j + 1];
                    typedef __bf16 bf2v __attribute__((ext_vector_type(2)));
                    bf2v t2; t2[0] = (__bf16)a0; t2[1] = (__bf16)a1;
                    pw[j] = __builtin_bit_cast(unsigned, t2);
                }
                const bf16x8 pf = __builtin_bit_cast(bf16x8, pw);
                const int q = (lane & 15) >> 2, p = lane & 3, cb = ((lane >> 4) & 1) * 16 + 4 * p;
                const u16* vb = &sV[buf][(c * 64 + ks * 16 + 4 * hi + q) * KSTR + cb];
                s16x4 lo = __builtin_bit_cast(s16x4, __builtin_amdgcn_ds_read_tr16_b64_v4i16((__attribute__((address_space(3))) v4i16_t*)vb));
                s16x4 hi4 = __builtin_bit_cast(s16x4, __builtin_amdgcn_ds_read_tr16_b64_v4i16((__attribute__((address_space(3))) v4i16_t*)(vb + 8 * KSTR)));
                bf16x8 vf = bf16x8{lo[0], lo[1], lo[2], lo[3], hi4[0], hi4[1], hi4[2], hi4[3]};
                o = __builtin_amdgcn_mfma_f32_32x32x16_bf16(vf, pf, o, 0, 0, 0);
            }
        }
        if (t + 1 < NT) { if (buf == 0) store_tiles(1, rkB, rvB); else store_tiles(0, rkA, rvA); }
        __syncthreads();
    }
    {
        float* sc = reinterpret_cast<float*>(&sKV[0][0][0][0]);
        if (grp == 1) {
            sc[tid] = mrun;
#pragma unroll
            for (int r = 0; r < 16; ++r) sc[(r + 1) * 256 + tid] = o[r];
        }
        __syncthreads();
        if (grp == 1) return;
        const float m2 = sc[tid];
        const float mn = fmaxf(mrun, m2);
        const float a1 = __builtin_amdgcn_exp2f((mrun - mn) * C2), a2 = __builtin_amdgcn_exp2f((m2 - mn) * C2);
#pragma unroll
        for (int r = 0; r < 16; ++r) o[r] = o[r] * a1 + sc[(r + 1) * 256 + tid] * a2;
    }
    const float l = __shfl(o[10], r32);
    const float inv = 1.f / l;
    u16* orow = O + (size_t)(q0 + r32) * DM + hh * DH;
#pragma unroll
    for (int r = 0; r < 16; ++r) {
        const int d = (r & 3) + 8 * (r >> 2) + 4 * hi;
        if (d < DH) orow[d] = f2bf(o[r] * inv);
    }
}

extern "C" void kernel_launch(void* const* d_in, const int* in_sizes, int n_in,
                              void* d_out, int out_size, void* d_ws, size_t ws_size,
                              hipStream_t stream) {
    const u16* x = (const u16*)d_in[0];
    const u16* wqkv = (const u16*)d_in[1];
    const u16* wo = (const u16*)d_in[2];
    const u16* w1 = (const u16*)d_in[4];
    const u16* w2 = (const u16*)d_in[5];
    const u16* w3 = (const u16*)d_in[6];
    const u16* w4 = (const u16*)d_in[7];
    u16* out = (u16*)d_out;

    const size_t T = (size_t)SEQ * DM;
    u16* ws = (u16*)d_ws;
    u16* qkv = ws;
    u16* xn = qkv + 3 * T;
    u16* Ob = xn + T;
    u16* hb = Ob + T;
    u16* yb = hb + T;
    u16* zb = yb + T;
    u16* hmp = zb + T;
    u16* hm = hmp + 2 * DM;
    u16* x1 = hmp + T + 4 * DM;
    u16* x1n = x1 + T;
    u16* gb = x1n + T;

    ln_k<false><<<SEQ / 4, 256, 0, stream>>>(x, xn, nullptr, nullptr);
    gemm_k<128, 64, 3, EPI_PLAIN, false><<<dim3(SEQ / 128, 3 * DM / 64), 256, 0, stream>>>(GemmArgs{xn, wqkv, qkv, nullptr, nullptr, 3 * DM, DM});
    attn_k<<<dim3(SEQ / 128, NH), 512, 0, stream>>>(qkv, Ob);
    gemm_k<64, 64, 3, EPI_RES, false><<<dim3(SEQ / 64, DM / 64), 256, 0, stream>>>(GemmArgs{Ob, wo, hb, x, nullptr, DM, DM});
    ln_k<true><<<SEQ / 4, 256, 0, stream>>>(hb, yb, zb, hmp);
    gemm_k<64, 64, 3, EPI_GELU, false><<<dim3(SEQ / 64, DM / 64), 256, 0, stream>>>(GemmArgs{zb, w1, hm, nullptr, nullptr, DM, DM});
    gemm_k<64, 64, 3, EPI_RES, true><<<dim3(SEQ / 64, DM / 64), 256, 0, stream>>>(GemmArgs{hm, w2, x1, yb, nullptr, DM, DM});
    ln_k<false><<<SEQ / 4, 256, 0, stream>>>(x1, x1n, nullptr, nullptr);
    gemm_k<128, 64, 3, EPI_GELU, false><<<dim3(SEQ / 128, FF / 64), 256, 0, stream>>>(GemmArgs{x1n, w3, gb, nullptr, nullptr, FF, DM});
    gemm_k<64, 64, 4, EPI_RES2, false><<<dim3(SEQ / 64, DM / 64), 256, 0, stream>>>(GemmArgs{gb, w4, out, x1, hb, DM, FF});
}
```

```cpp
#include <hip/hip_runtime.h>
#include <hip/hip_bf16.h>
#include <stdint.h>

static constexpr int SEQ = 2048, DM = 576, NH = 32, DH = 18, FF = 4608;
static constexpr int DP = 32;

using bf16x8 = __attribute__((ext_vector_type(8))) short;
using s16x4 = __attribute__((ext_vector_type(4))) short;
using f32x4 = __attribute__((ext_vector_type(4))) float;
using f32x16 = __attribute__((ext_vector_type(16))) float;
using u32x4 = __attribute__((ext_vector_type(4))) unsigned;
typedef short v4i16_t __attribute__((ext_vector_type(4)));
typedef unsigned short u16;

__device__ __forceinline__ float bf2f(u16 v) { return __uint_as_float(((unsigned)v) << 16); }
__device__ __forceinline__ u16 f2bf(float f) {
    unsigned u = __float_as_uint(f);
    u += 0x7FFF + ((u >> 16) & 1);
    return (u16)(u >> 16);
}
__device__ __forceinline__ float rbf(float f) { return bf2f(f2bf(f)); }

__device__ __forceinline__ float gelu_tanh(float x) {
    const float u = 0.7978845608028654f * (x + 0.044715f * x * x * x);
    const float e = __expf(2.f * u);
    const float t = 1.f - 2.f / (e + 1.f);
    return 0.5f * x * (1.f + t);
}


struct GemmArgs {
    const u16* A;
    const u16* B;
    u16* C;
    const u16* R1;
    const u16* R2;
    int N;
    int K;
};
enum { EPI_PLAIN = 0, EPI_RES = 1, EPI_GELU = 2, EPI_RES2 = 3, EPI_PART = 4 };
static constexpr int SPLITK = 4;

template <int BN>
__device__ __forceinline__ int swzB(int row) {
    if constexpr (BN == 64) return ((((row >> 1) & 1) | (((row >> 3) & 1) << 1)) << 1);
    else return (((row & 3) << 1) | (((row >> 3) & 1) << 3));
}

template <int BM, int BN, int NS, int EPI, bool SHIFT>
__global__ __launch_bounds__(256) void gemm_k(GemmArgs a) {
    constexpr int BK = 64;
    constexpr int WM = BM / 2, WN = BN / 2, TM = WM / 16, TN = WN / 16;
    constexpr int ACH = BM * 8 / 256, BCH = BK * BN / 8 / 256, L = ACH + BCH;
    constexpr int ASZ = BM * BK, BSZ = BK * BN, STG = ASZ + BSZ;
    constexpr int CSTR = BN + 8;
    static_assert(BM * CSTR <= NS * STG, "epilogue tile must fit");
    __shared__ __attribute__((aligned(16))) u16 smem[NS * STG];

    const int tid = threadIdx.x, lane = tid & 63, wid = tid >> 6;
    const int wm = wid >> 1, wn = wid & 1;
    const int lb = blockIdx.y * gridDim.x + blockIdx.x, jj = lb >> 3;
    const int m0 = ((lb & 7) + 8 * (jj / (int)gridDim.y)) * BM, n0 = (jj % (int)gridDim.y) * BN;
    const int K = a.K, N = a.N, nk = (EPI == EPI_PART) ? K / BK / SPLITK : K / BK;
    const int kofs = (EPI == EPI_PART) ? blockIdx.z * (K / SPLITK) : 0;

    const u16* asrc[ACH];
    const u16* bsrc[BCH];
#pragma unroll
    for (int i = 0; i < ACH; ++i) {
        const int idx = tid + i * 256, row = idx >> 3, ch = (idx & 7) ^ (row & 7);
        asrc[i] = a.A + (size_t)(m0 + row) * K + ch * 8 + kofs;
    }
#pragma unroll
    for (int i = 0; i < BCH; ++i) {
        const int idx = tid + i * 256, row = idx / (BN / 8), ch = (idx % (BN / 8)) ^ swzB<BN>(row);
        bsrc[i] = a.B + (size_t)(row + kofs) * N + n0 + ch * 8;
    }
    auto issue = [&](int kt, int slot) {
        u16* st = smem + slot * STG;
#pragma unroll
        for (int i = 0; i < ACH; ++i) {
            long off = (long)kt * BK;
            if constexpr (SHIFT) {
                const int col = kt * BK + (((tid + i * 256) & 7) ^ (((tid + i * 256) >> 3) & 7)) * 8;
                const int grp = col / 144;
                off += (long)K * ((grp == 0) ? -1 : (grp == 1) ? 1 : (grp == 2) ? -2 : 2);
            }
            __builtin_amdgcn_global_load_lds((const unsigned*)(asrc[i] + off), (unsigned*)(st + (tid + i * 256) * 8), 16, 0, 0);
        }
#pragma unroll
        for (int i = 0; i < BCH; ++i)
            __builtin_amdgcn_global_load_lds((const unsigned*)(bsrc[i] + (size_t)kt * BK * N), (unsigned*)(st + ASZ + (tid + i * 256) * 8), 16, 0, 0);
    };

    f32x4 acc[TM][TN];
#pragma unroll
    for (int i = 0; i < TM; ++i)
#pragma unroll
        for (int j = 0; j < TN; ++j) acc[i][j] = f32x4{0.f, 0.f, 0.f, 0.f};

#pragma unroll
    for (int s = 0; s < NS - 1; ++s) if (s < nk) issue(s, s);

    for (int kt0 = 0; kt0 < nk; kt0 += NS)
#pragma unroll
    for (int sl = 0; sl < NS; ++sl) {
        const int kt = kt0 + sl;
        if (kt + NS - 2 < nk) asm volatile("s_waitcnt vmcnt(%0) lgkmcnt(0)" :: "i"((NS - 2) * L) : "memory");
        else asm volatile("s_waitcnt vmcnt(0) lgkmcnt(0)" ::: "memory");
        __builtin_amdgcn_s_barrier();
        asm volatile("" ::: "memory");
        if (kt + NS - 1 < nk) issue(kt + NS - 1, (sl + NS - 1) % NS);
        const u16* sA = smem + sl * STG;
        const u16* sB = sA + ASZ;
#pragma unroll
        for (int kk = 0; kk < 2; ++kk) {
            bf16x8 af[TM], bfr[TN];
            s16x4 blo[TN], bhi[TN];
#pragma unroll
            for (int i = 0; i < TM; ++i) {
                const int row = wm * WM + i * 16 + (lane & 15);
                af[i] = *reinterpret_cast<const bf16x8*>(sA + row * BK + ((kk * 4 + (lane >> 4)) ^ (row & 7)) * 8);
            }
#pragma unroll
            for (int j = 0; j < TN; ++j) {
                const int g = lane >> 4, q = (lane & 15) >> 2, p = lane & 3;
                const int r0 = kk * 32 + 8 * g + q, r1 = r0 + 4;
                const int ch = (wn * WN + j * 16) / 8 + (p >> 1);
                const u16* p0 = sB + r0 * BN + (ch ^ swzB<BN>(r0)) * 8 + (p & 1) * 4;
                const u16* p1 = sB + r1 * BN + (ch ^ swzB<BN>(r1)) * 8 + (p & 1) * 4;
                s16x4 lo, hi;
                asm volatile("ds_read_b64_tr_b16 %0, %1" : "=v"(lo) : "v"((unsigned)(uintptr_t)p0) : "memory");
                asm volatile("ds_read_b64_tr_b16 %0, %1" : "=v"(hi) : "v"((unsigned)(uintptr_t)p1) : "memory");
                blo[j] = lo; bhi[j] = hi;
            }
#pragma unroll
            for (int j = 0; j < TN; ++j) asm volatile("s_waitcnt lgkmcnt(0)" : "+v"(blo[j]), "+v"(bhi[j]) :: "memory");
#pragma unroll
            for (int j = 0; j < TN; ++j) {
                s16x4 lo = blo[j], hi = bhi[j];
                bfr[j] = bf16x8{lo[0], lo[1], lo[2], lo[3], hi[0], hi[1], hi[2], hi[3]};
            }
#pragma unroll
            for (int i = 0; i < TM; ++i)
#pragma unroll
                for (int j = 0; j < TN; ++j)
                    acc[i][j] = __builtin_amdgcn_mfma_f32_16x16x32_bf16(af[i], bfr[j], acc[i][j], 0, 0, 0);
        }
    }
    if constexpr (EPI == EPI_PART) {
        float* P = reinterpret_cast<float*>(a.C) + (size_t)blockIdx.z * SEQ * N;
#pragma unroll
        for (int i = 0; i < TM; ++i)
#pragma unroll
            for (int j = 0; j < TN; ++j)
#pragma unroll
                for (int r = 0; r < 4; ++r)
                    P[(size_t)(m0 + wm * WM + i * 16 + (lane >> 4) * 4 + r) * N + n0 + wn * WN + j * 16 + (lane & 15)] = acc[i][j][r];
        return;
    }
    __syncthreads();
#pragma unroll
    for (int i = 0; i < TM; ++i)
#pragma unroll
        for (int j = 0; j < TN; ++j)
#pragma unroll
            for (int r = 0; r < 4; ++r)
                smem[(wm * WM + i * 16 + (lane >> 4) * 4 + r) * CSTR + wn * WN + j * 16 + (lane & 15)] = f2bf(acc[i][j][r]);
    __syncthreads();
    constexpr int CCH = BM * BN / 8 / 256;
#pragma unroll
    for (int i = 0; i < CCH; ++i) {
        const int idx = tid + i * 256, row = idx / (BN / 8), ch = idx % (BN / 8);
        bf16x8 v = *reinterpret_cast<const bf16x8*>(smem + row * CSTR + ch * 8);
        const size_t go = (size_t)(m0 + row) * N + n0 + ch * 8;
        if constexpr (EPI == EPI_RES || EPI == EPI_RES2) {
            bf16x8 r1 = *reinterpret_cast<const bf16x8*>(a.R1 + go);
#pragma unroll
            for (int j = 0; j < 8; ++j) v[j] = (short)f2bf(bf2f((u16)r1[j]) + bf2f((u16)v[j]));
            if constexpr (EPI == EPI_RES2) {
                bf16x8 r2 = *reinterpret_cast<const bf16x8*>(a.R2 + go);
#pragma unroll
                for (int j = 0; j < 8; ++j) v[j] = (short)f2bf(bf2f((u16)r2[j]) + bf2f((u16)v[j]));
            }
        } else if constexpr (EPI == EPI_GELU) {
#pragma unroll
            for (int j = 0; j < 8; ++j) v[j] = (short)f2bf(gelu_tanh(bf2f((u16)v[j])));
        }
        *reinterpret_cast<bf16x8*>(a.C + go) = v;
    }
}

__global__ __launch_bounds__(256) void redk_k(const float* __restrict__ P, const u16* __restrict__ R1, const u16* __restrict__ R2, u16* __restrict__ out) {
    const size_t i = ((size_t)blockIdx.x * 256 + threadIdx.x) * 4;
    f32x4 s = *reinterpret_cast<const f32x4*>(P + i);
#pragma unroll
    for (int z = 1; z < SPLITK; ++z) s += *reinterpret_cast<const f32x4*>(P + (size_t)z * SEQ * DM + i);
    s16x4 r1 = *reinterpret_cast<const s16x4*>(R1 + i), r2 = *reinterpret_cast<const s16x4*>(R2 + i), o;
#pragma unroll
    for (int j = 0; j < 4; ++j) o[j] = (short)f2bf(bf2f((u16)r2[j]) + rbf(bf2f((u16)r1[j]) + rbf(s[j])));
    *reinterpret_cast<s16x4*>(out + i) = o;
}

__device__ __forceinline__ float wsum(float v) {
#pragma unroll
    for (int o = 32; o > 0; o >>= 1) v += __shfl_xor(v, o);
    return v;
}
__device__ __forceinline__ void ln_regs(float (&v)[16], int cnt) {
    float s = 0.f;
#pragma unroll
    for (int j = 0; j < 16; ++j) s += (j < cnt) ? v[j] : 0.f;
    const float mean = wsum(s) * (1.f / DM);
    float q = 0.f;
#pragma unroll
    for (int j = 0; j < 16; ++j) { const float d = v[j] - mean; q += (j < cnt) ? d * d : 0.f; }
    const float var = wsum(q) * (1.f / DM);
    const float mb = rbf(mean), sd = rbf(sqrtf(rbf(rbf(var) + 1e-5f)));
#pragma unroll
    for (int j = 0; j < 16; ++j) v[j] = rbf(rbf(v[j] - mb) / sd);
}
template <bool DOUBLE>
__global__ __launch_bounds__(256) void ln_k(const u16* __restrict__ X, u16* __restrict__ Y, u16* __restrict__ Z, u16* __restrict__ pad) {
    const int lane = threadIdx.x & 63, row = blockIdx.x * 4 + (threadIdx.x >> 6);
    const int cnt = lane < 8 ? 16 : 8;
    float v[16];
    bf16x8 t0 = *reinterpret_cast<const bf16x8*>(X + (size_t)row * DM + lane * 8);
    bf16x8 t1 = t0;
    if (lane < 8) t1 = *reinterpret_cast<const bf16x8*>(X + (size_t)row * DM + 512 + lane * 8);
#pragma unroll
    for (int j = 0; j < 8; ++j) { v[j] = bf2f((u16)t0[j]); v[8 + j] = bf2f((u16)t1[j]); }
    ln_regs(v, cnt);
#pragma unroll
    for (int j = 0; j < 8; ++j) { t0[j] = (short)f2bf(v[j]); t1[j] = (short)f2bf(v[8 + j]); }
    *reinterpret_cast<bf16x8*>(Y + (size_t)row * DM + lane * 8) = t0;
    if (lane < 8) *reinterpret_cast<bf16x8*>(Y + (size_t)row * DM + 512 + lane * 8) = t1;
    if constexpr (DOUBLE) {
        ln_regs(v, cnt);
#pragma unroll
        for (int j = 0; j < 8; ++j) { t0[j] = (short)f2bf(v[j]); t1[j] = (short)f2bf(v[8 + j]); }
        *reinterpret_cast<bf16x8*>(Z + (size_t)row * DM + lane * 8) = t0;
        if (lane < 8) *reinterpret_cast<bf16x8*>(Z + (size_t)row * DM + 512 + lane * 8) = t1;
        if (blockIdx.x == 0) {
            for (int i = threadIdx.x; i < 2 * DM; i += 256) { pad[i] = 0; pad[(size_t)(SEQ + 2) * DM + i] = 0; }
        }
    }
}

static constexpr int KC = 128;
static constexpr int KSTR = 40;
static constexpr int QKVS = 3 * DM;

__device__ __forceinline__ float max3f(float a, float b, float c) { return __builtin_fmaxf(__builtin_fmaxf(a, b), c); }
__global__ __launch_bounds__(512, 4) void attn_k(const u16* __restrict__ qkv, u16* __restrict__ O) {
    __shared__ __attribute__((aligned(16))) u16 sKV[2][2][2][KC * KSTR];
    const int grp = threadIdx.x >> 8;
    u16 (*sK)[KC * KSTR] = sKV[grp][0];
    u16 (*sV)[KC * KSTR] = sKV[grp][1];
    const int tid = threadIdx.x & 255, lane = tid & 63, wid = tid >> 6;
    const int r32 = lane & 31, hi = lane >> 5;
    const int hh = blockIdx.y;
    const int q0 = blockIdx.x * 128 + wid * 32;

    for (int i = tid; i < 2 * KC * 7; i += 256) {
        const int b = i / (KC * 7), rem = i % (KC * 7), r = rem / 7, c = 18 + (rem % 7) * 2;
        *reinterpret_cast<unsigned*>(&sK[b][r * KSTR + c]) = 0u;
        *reinterpret_cast<unsigned*>(&sV[b][r * KSTR + c]) = (c == 18) ? 0x3F80u : 0u;
    }
    bf16x8 qf[2];
    {
        const unsigned* qp = reinterpret_cast<const unsigned*>(qkv + (size_t)(q0 + r32) * QKVS + hh * DH);
        u32x4 w0 = hi ? u32x4{qp[4], qp[5], qp[6], qp[7]} : u32x4{qp[0], qp[1], qp[2], qp[3]};
        u32x4 w1 = hi ? u32x4{0u, 0u, 0u, 0u} : u32x4{qp[8], 0u, 0u, 0u};
        qf[0] = __builtin_bit_cast(bf16x8, w0);
        qf[1] = __builtin_bit_cast(bf16x8, w1);
    }
    const float C2 = 0.23570226039551584f * 1.4426950408889634f;
    float mrun = -INFINITY;
    f32x16 o = f32x16{};

    unsigned rkA[5], rvA[5], rkB[5], rvB[5];
    auto load_tiles = [&](int k0, unsigned (&rk)[5], unsigned (&rv)[5]) {
#pragma unroll
        for (int i = 0; i < 5; ++i) {
            const int idx = tid + i * 256;
            if (idx < KC * 9) {
                const int r = idx / 9, c = idx % 9;
                const unsigned* p = reinterpret_cast<const unsigned*>(qkv + (size_t)(k0 + r) * QKVS + DM + hh * DH) + c;
                rk[i] = p[0];
                rv[i] = p[DM / 2];
            }
        }
    };
    auto store_tiles = [&](int buf, unsigned (&rk)[5], unsigned (&rv)[5]) {
#pragma unroll
        for (int i = 0; i < 5; ++i) {
            const int idx = tid + i * 256;
            if (idx < KC * 9) {
                const int r = idx / 9, c = idx % 9;
                *reinterpret_cast<unsigned*>(&sK[buf][r * KSTR + c * 2]) = rk[i];
                *reinterpret_cast<unsigned*>(&sV[buf][r * KSTR + c * 2]) = rv[i];
            }
        }
    };

    constexpr int NT = SEQ / KC / 2;
    load_tiles(grp * KC, rkA, rvA);
    load_tiles((2 + grp) * KC, rkB, rvB);
    store_tiles(0, rkA, rvA);
    __syncthreads();
#pragma unroll 2
    for (int t = 0; t < NT; ++t) {
        const int buf = t & 1;
        if (t + 2 < NT) { if (buf == 0) load_tiles((2 * (t + 2) + grp) * KC, rkA, rvA); else load_tiles((2 * (t + 2) + grp) * KC, rkB, rvB); }
#pragma unroll
        for (int c = 0; c < KC / 64; ++c) {
            f32x16 s0 = f32x16{}, s1 = f32x16{};
            {
                const u16* kb = &sK[buf][(c * 64 + r32) * KSTR + hi * 8];
                bf16x8 k00 = *reinterpret_cast<const bf16x8*>(kb);
                bf16x8 k01 = *reinterpret_cast<const bf16x8*>(kb + 16);
                bf16x8 k10 = *reinterpret_cast<const bf16x8*>(kb + 32 * KSTR);
                bf16x8 k11 = *reinterpret_cast<const bf16x8*>(kb + 32 * KSTR + 16);
                s0 = __builtin_amdgcn_mfma_f32_32x32x16_bf16(k00, qf[0], s0, 0, 0, 0);
                s1 = __builtin_amdgcn_mfma_f32_32x32x16_bf16(k10, qf[0], s1, 0, 0, 0);
                s0 = __builtin_amdgcn_mfma_f32_32x32x16_bf16(k01, qf[1], s0, 0, 0, 0);
                s1 = __builtin_amdgcn_mfma_f32_32x32x16_bf16(k11, qf[1], s1, 0, 0, 0);
            }
            float tma = max3f(s0[0], s1[0], s0[1]), tmb = max3f(s1[1], s0[2], s1[2]);
#pragma unroll
            for (int r = 3; r < 15; r += 2) { tma = max3f(tma, s0[r], s1[r]); tmb = max3f(tmb, s0[r + 1], s1[r + 1]); }
            float tm = max3f(tma, tmb, max3f(s0[15], s1[15], mrun));
            {
                auto rr = __builtin_amdgcn_permlane32_swap(__float_as_uint(tm), __float_as_uint(tm), false, false);
                tm = max3f(__uint_as_float(rr[0]), __uint_as_float(rr[1]), mrun);
            }
            if (!__all(tm <= mrun)) {
                const float mnew = tm;
                const float alpha = __builtin_amdgcn_exp2f((mrun - mnew) * C2);
                mrun = mnew;
#pragma unroll
                for (int r = 0; r < 16; ++r) o[r] *= alpha;
            }
            const float nm = -mrun * C2;
#pragma unroll
            for (int r = 0; r < 16; ++r) {
                s0[r] = __builtin_amdgcn_exp2f(fmaf(s0[r], C2, nm));
                s1[r] = __builtin_amdgcn_exp2f(fmaf(s1[r], C2, nm));
            }
#pragma unroll
            for (int ks = 0; ks < 4; ++ks) {
                u32x4 pw;
#pragma unroll
                for (int j = 0; j < 4; ++j) {
                    const float a0 = (ks < 2) ? s0[(ks & 1) * 8 + 2 * j] : s1[(ks & 1) * 8 + 2 * j];
                    const float a1 = (ks < 2) ? s0[(ks & 1) * 8 + 2 * j + 1] : s1[(ks & 1) * 8 + 2 * j + 1];
                    typedef __bf16 bf2v __attribute__((ext_vector_type(2)));
                    bf2v t2; t2[0] = (__bf16)a0; t2[1] = (__bf16)a1;
                    pw[j] = __builtin_bit_cast(unsigned, t2);
                }
                const bf16x8 pf = __builtin_bit_cast(bf16x8, pw);
                const int q = (lane & 15) >> 2, p = lane & 3, cb = ((lane >> 4) & 1) * 16 + 4 * p;
                const u16* vb = &sV[buf][(c * 64 + ks * 16 + 4 * hi + q) * KSTR + cb];
                s16x4 lo = __builtin_bit_cast(s16x4, __builtin_amdgcn_ds_read_tr16_b64_v4i16((__attribute__((address_space(3))) v4i16_t*)vb));
                s16x4 hi4 = __builtin_bit_cast(s16x4, __builtin_amdgcn_ds_read_tr16_b64_v4i16((__attribute__((address_space(3))) v4i16_t*)(vb + 8 * KSTR)));
                bf16x8 vf = bf16x8{lo[0], lo[1], lo[2], lo[3], hi4[0], hi4[1], hi4[2], hi4[3]};
                o = __builtin_amdgcn_mfma_f32_32x32x16_bf16(vf, pf, o, 0, 0, 0);
            }
        }
        if (t + 1 < NT) { if (buf == 0) store_tiles(1, rkB, rvB); else store_tiles(0, rkA, rvA); }
        __syncthreads();
    }
    {
        float* sc = reinterpret_cast<float*>(&sKV[0][0][0][0]);
        if (grp == 1) {
            sc[tid] = mrun;
#pragma unroll
            for (int r = 0; r < 16; ++r) sc[(r + 1) * 256 + tid] = o[r];
        }
        __syncthreads();
        if (grp == 1) return;
        const float m2 = sc[tid];
        const float mn = fmaxf(mrun, m2);
        const float a1 = __builtin_amdgcn_exp2f((mrun - mn) * C2), a2 = __builtin_amdgcn_exp2f((m2 - mn) * C2);
#pragma unroll
        for (int r = 0; r < 16; ++r) o[r] = o[r] * a1 + sc[(r + 1) * 256 + tid] * a2;
    }
    const float l = __shfl(o[10], r32);
    const float inv = 1.f / l;
    u16* orow = O + (size_t)(q0 + r32) * DM + hh * DH;
#pragma unroll
    for (int r = 0; r < 16; ++r) {
        const int d = (r & 3) + 8 * (r >> 2) + 4 * hi;
        if (d < DH) orow[d] = f2bf(o[r] * inv);
    }
}

extern "C" void kernel_launch(void* const* d_in, const int* in_sizes, int n_in,
                              void* d_out, int out_size, void* d_ws, size_t ws_size,
                              hipStream_t stream) {
    const u16* x = (const u16*)d_in[0];
    const u16* wqkv = (const u16*)d_in[1];
    const u16* wo = (const u16*)d_in[2];
    const u16* w1 = (const u16*)d_in[4];
    const u16* w2 = (const u16*)d_in[5];
    const u16* w3 = (const u16*)d_in[6];
    const u16* w4 = (const u16*)d_in[7];
    u16* out = (u16*)d_out;

    const size_t T = (size_t)SEQ * DM;
    u16* ws = (u16*)d_ws;
    u16* qkv = ws;
    u16* xn = qkv + 3 * T;
    u16* Ob = xn + T;
    u16* hb = Ob + T;
    u16* yb = hb + T;
    u16* zb = yb + T;
    u16* hmp = zb + T;
    u16* hm = hmp + 2 * DM;
    u16* x1 = hmp + T + 4 * DM;
    u16* x1n = x1 + T;
    u16* gb = x1n + T;

    ln_k<false><<<SEQ / 4, 256, 0, stream>>>(x, xn, nullptr, nullptr);
    gemm_k<128, 64, 3, EPI_PLAIN, false><<<dim3(SEQ / 128, 3 * DM / 64), 256, 0, stream>>>(GemmArgs{xn, wqkv, qkv, nullptr, nullptr, 3 * DM, DM});
    attn_k<<<dim3(SEQ / 128, NH), 512, 0, stream>>>(qkv, Ob);
    gemm_k<64, 64, 3, EPI_RES, false><<<dim3(SEQ / 64, DM / 64), 256, 0, stream>>>(GemmArgs{Ob, wo, hb, x, nullptr, DM, DM});
    ln_k<true><<<SEQ / 4, 256, 0, stream>>>(hb, yb, zb, hmp);
    gemm_k<64, 64, 3, EPI_GELU, false><<<dim3(SEQ / 64, DM / 64), 256, 0, stream>>>(GemmArgs{zb, w1, hm, nullptr, nullptr, DM, DM});
    gemm_k<64, 64, 3, EPI_RES, true><<<dim3(SEQ / 64, DM / 64), 256, 0, stream>>>(GemmArgs{hm, w2, x1, yb, nullptr, DM, DM});
    ln_k<false><<<SEQ / 4, 256, 0, stream>>>(x1, x1n, nullptr, nullptr);
    gemm_k<128, 64, 3, EPI_GELU, false><<<dim3(SEQ / 128, FF / 64), 256, 0, stream>>>(GemmArgs{x1n, w3, gb, nullptr, nullptr, FF, DM});
    u16* pk = gb + (size_t)SEQ * FF;
    gemm_k<64, 64, 3, EPI_PART, false><<<dim3(SEQ / 64, DM / 64, SPLITK), 256, 0, stream>>>(GemmArgs{gb, w4, pk, nullptr, nullptr, DM, FF});
    redk_k<<<SEQ * DM / 1024, 256, 0, stream>>>((const float*)pk, x1, hb, out);
}
```

```cpp
#include <hip/hip_runtime.h>
#include <hip/hip_bf16.h>
#include <stdint.h>

static constexpr int SEQ = 2048, DM = 576, NH = 32, DH = 18, FF = 4608;
static constexpr int DP = 32;

using bf16x8 = __attribute__((ext_vector_type(8))) short;
using s16x4 = __attribute__((ext_vector_type(4))) short;
using f32x4 = __attribute__((ext_vector_type(4))) float;
using f32x16 = __attribute__((ext_vector_type(16))) float;
using u32x4 = __attribute__((ext_vector_type(4))) unsigned;
typedef short v4i16_t __attribute__((ext_vector_type(4)));
typedef unsigned short u16;

__device__ __forceinline__ float bf2f(u16 v) { return __uint_as_float(((unsigned)v) << 16); }
__device__ __forceinline__ u16 f2bf(float f) {
    unsigned u = __float_as_uint(f);
    u += 0x7FFF + ((u >> 16) & 1);
    return (u16)(u >> 16);
}
__device__ __forceinline__ float rbf(float f) { return bf2f(f2bf(f)); }

__device__ __forceinline__ float gelu_tanh(float x) {
    const float u = 0.7978845608028654f * (x + 0.044715f * x * x * x);
    const float e = __expf(2.f * u);
    const float t = 1.f - 2.f / (e + 1.f);
    return 0.5f * x * (1.f + t);
}


struct GemmArgs {
    const u16* A;
    const u16* B;
    u16* C;
    const u16* R1;
    const u16* R2;
    int N;
    int K;
};
enum { EPI_PLAIN = 0, EPI_RES = 1, EPI_GELU = 2, EPI_RES2 = 3, EPI_PART = 4 };
static constexpr int SPLITK = 6;

template <int BN>
__device__ __forceinline__ int swzB(int row) {
    if constexpr (BN != 128) return ((((row >> 1) & 1) | (((row >> 3) & 1) << 1)) << 1);
    else return (((row & 3) << 1) | (((row >> 3) & 1) << 3));
}

template <int BM, int BN, int NS, int EPI, bool SHIFT>
__global__ __launch_bounds__(256) void gemm_k(GemmArgs a) {
    constexpr int BK = 64;
    constexpr int WM = BM / 2, WN = BN / 2, TM = WM / 16, TN = WN / 16;
    constexpr int ACH = BM * 8 / 256, BCH = BK * BN / 8 / 256, L = ACH + BCH;
    constexpr int ASZ = BM * BK, BSZ = BK * BN, STG = ASZ + BSZ;
    constexpr int CSTR = BN + 8;
    static_assert(BM * CSTR <= NS * STG, "epilogue tile must fit");
    __shared__ __attribute__((aligned(16))) u16 smem[NS * STG];

    const int tid = threadIdx.x, lane = tid & 63, wid = tid >> 6;
    const int wm = wid >> 1, wn = wid & 1;
    const int lb = blockIdx.y * gridDim.x + blockIdx.x, jj = lb >> 3;
    const int m0 = ((lb & 7) + 8 * (jj / (int)gridDim.y)) * BM, n0 = (jj % (int)gridDim.y) * BN;
    const int K = a.K, N = a.N, nk = (EPI == EPI_PART) ? K / BK / SPLITK : K / BK;
    const int kofs = (EPI == EPI_PART) ? blockIdx.z * (K / SPLITK) : 0;

    const u16* asrc[ACH];
    const u16* bsrc[BCH];
#pragma unroll
    for (int i = 0; i < ACH; ++i) {
        const int idx = tid + i * 256, row = idx >> 3, ch = (idx & 7) ^ (row & 7);
        asrc[i] = a.A + (size_t)(m0 + row) * K + ch * 8 + kofs;
    }
#pragma unroll
    for (int i = 0; i < BCH; ++i) {
        const int idx = tid + i * 256, row = idx / (BN / 8), ch = (idx % (BN / 8)) ^ swzB<BN>(row);
        bsrc[i] = a.B + (size_t)(row + kofs) * N + n0 + ch * 8;
    }
    auto issue = [&](int kt, int slot) {
        u16* st = smem + slot * STG;
#pragma unroll
        for (int i = 0; i < ACH; ++i) {
            long off = (long)kt * BK;
            if constexpr (SHIFT) {
                const int col = kt * BK + (((tid + i * 256) & 7) ^ (((tid + i * 256) >> 3) & 7)) * 8;
                const int grp = col / 144;
                off += (long)K * ((grp == 0) ? -1 : (grp == 1) ? 1 : (grp == 2) ? -2 : 2);
            }
            __builtin_amdgcn_global_load_lds((const unsigned*)(asrc[i] + off), (unsigned*)(st + (tid + i * 256) * 8), 16, 0, 0);
        }
#pragma unroll
        for (int i = 0; i < BCH; ++i)
            __builtin_amdgcn_global_load_lds((const unsigned*)(bsrc[i] + (size_t)kt * BK * N), (unsigned*)(st + ASZ + (tid + i * 256) * 8), 16, 0, 0);
    };

    f32x4 acc[TM][TN];
#pragma unroll
    for (int i = 0; i < TM; ++i)
#pragma unroll
        for (int j = 0; j < TN; ++j) acc[i][j] = f32x4{0.f, 0.f, 0.f, 0.f};

#pragma unroll
    for (int s = 0; s < NS - 1; ++s) if (s < nk) issue(s, s);

    for (int kt0 = 0; kt0 < nk; kt0 += NS)
#pragma unroll
    for (int sl = 0; sl < NS; ++sl) {
        const int kt = kt0 + sl;
        if (kt + NS - 2 < nk) asm volatile("s_waitcnt vmcnt(%0) lgkmcnt(0)" :: "i"((NS - 2) * L) : "memory");
        else asm volatile("s_waitcnt vmcnt(0) lgkmcnt(0)" ::: "memory");
        __builtin_amdgcn_s_barrier();
        asm volatile("" ::: "memory");
        if (kt + NS - 1 < nk) issue(kt + NS - 1, (sl + NS - 1) % NS);
        const u16* sA = smem + sl * STG;
        const u16* sB = sA + ASZ;
#pragma unroll
        for (int kk = 0; kk < 2; ++kk) {
            bf16x8 af[TM], bfr[TN];
            s16x4 blo[TN], bhi[TN];
#pragma unroll
            for (int i = 0; i < TM; ++i) {
                const int row = wm * WM + i * 16 + (lane & 15);
                af[i] = *reinterpret_cast<const bf16x8*>(sA + row * BK + ((kk * 4 + (lane >> 4)) ^ (row & 7)) * 8);
            }
#pragma unroll
            for (int j = 0; j < TN; ++j) {
                const int g = lane >> 4, q = (lane & 15) >> 2, p = lane & 3;
                const int r0 = kk * 32 + 8 * g + q, r1 = r0 + 4;
                const int ch = (wn * WN + j * 16) / 8 + (p >> 1);
                const u16* p0 = sB + r0 * BN + (ch ^ swzB<BN>(r0)) * 8 + (p & 1) * 4;
                const u16* p1 = sB + r1 * BN + (ch ^ swzB<BN>(r1)) * 8 + (p & 1) * 4;
                s16x4 lo, hi;
                asm volatile("ds_read_b64_tr_b16 %0, %1" : "=v"(lo) : "v"((unsigned)(uintptr_t)p0) : "memory");
                asm volatile("ds_read_b64_tr_b16 %0, %1" : "=v"(hi) : "v"((unsigned)(uintptr_t)p1) : "memory");
                blo[j] = lo; bhi[j] = hi;
            }
#pragma unroll
            for (int j = 0; j < TN; ++j) asm volatile("s_waitcnt lgkmcnt(0)" : "+v"(blo[j]), "+v"(bhi[j]) :: "memory");
#pragma unroll
            for (int j = 0; j < TN; ++j) {
                s16x4 lo = blo[j], hi = bhi[j];
                bfr[j] = bf16x8{lo[0], lo[1], lo[2], lo[3], hi[0], hi[1], hi[2], hi[3]};
            }
#pragma unroll
            for (int i = 0; i < TM; ++i)
#pragma unroll
                for (int j = 0; j < TN; ++j)
                    acc[i][j] = __builtin_amdgcn_mfma_f32_16x16x32_bf16(af[i], bfr[j], acc[i][j], 0, 0, 0);
        }
    }
    if constexpr (EPI == EPI_PART) {
        float* P = reinterpret_cast<float*>(a.C) + (size_t)blockIdx.z * SEQ * N;
#pragma unroll
        for (int i = 0; i < TM; ++i)
#pragma unroll
            for (int j = 0; j < TN; ++j)
#pragma unroll
                for (int r = 0; r < 4; ++r)
                    P[(size_t)(m0 + wm * WM + i * 16 + (lane >> 4) * 4 + r) * N + n0 + wn * WN + j * 16 + (lane & 15)] = acc[i][j][r];
        return;
    }
    __syncthreads();
#pragma unroll
    for (int i = 0; i < TM; ++i)
#pragma unroll
        for (int j = 0; j < TN; ++j)
#pragma unroll
            for (int r = 0; r < 4; ++r)
                smem[(wm * WM + i * 16 + (lane >> 4) * 4 + r) * CSTR + wn * WN + j * 16 + (lane & 15)] = f2bf(acc[i][j][r]);
    __syncthreads();
    constexpr int CCH = BM * BN / 8 / 256;
#pragma unroll
    for (int i = 0; i < CCH; ++i) {
        const int idx = tid + i * 256, row = idx / (BN / 8), ch = idx % (BN / 8);
        bf16x8 v = *reinterpret_cast<const bf16x8*>(smem + row * CSTR + ch * 8);
        const size_t go = (size_t)(m0 + row) * N + n0 + ch * 8;
        if constexpr (EPI == EPI_RES || EPI == EPI_RES2) {
            bf16x8 r1 = *reinterpret_cast<const bf16x8*>(a.R1 + go);
#pragma unroll
            for (int j = 0; j < 8; ++j) v[j] = (short)f2bf(bf2f((u16)r1[j]) + bf2f((u16)v[j]));
            if constexpr (EPI == EPI_RES2) {
                bf16x8 r2 = *reinterpret_cast<const bf16x8*>(a.R2 + go);
#pragma unroll
                for (int j = 0; j < 8; ++j) v[j] = (short)f2bf(bf2f((u16)r2[j]) + bf2f((u16)v[j]));
            }
        } else if constexpr (EPI == EPI_GELU) {
#pragma unroll
            for (int j = 0; j < 8; ++j) v[j] = (short)f2bf(gelu_tanh(bf2f((u16)v[j])));
        }
        *reinterpret_cast<bf16x8*>(a.C + go) = v;
    }
}

__global__ __launch_bounds__(256) void redk_k(const float* __restrict__ P, const u16* __restrict__ R1, const u16* __restrict__ R2, u16* __restrict__ out) {
    const size_t i = ((size_t)blockIdx.x * 256 + threadIdx.x) * 4;
    f32x4 s = *reinterpret_cast<const f32x4*>(P + i);
#pragma unroll
    for (int z = 1; z < SPLITK; ++z) s += *reinterpret_cast<const f32x4*>(P + (size_t)z * SEQ * DM + i);
    s16x4 r1 = *reinterpret_cast<const s16x4*>(R1 + i), r2 = *reinterpret_cast<const s16x4*>(R2 + i), o;
#pragma unroll
    for (int j = 0; j < 4; ++j) o[j] = (short)f2bf(bf2f((u16)r2[j]) + rbf(bf2f((u16)r1[j]) + rbf(s[j])));
    *reinterpret_cast<s16x4*>(out + i) = o;
}

__device__ __forceinline__ float wsum(float v) {
#pragma unroll
    for (int o = 32; o > 0; o >>= 1) v += __shfl_xor(v, o);
    return v;
}
__device__ __forceinline__ void ln_regs(float (&v)[16], int cnt) {
    float s = 0.f;
#pragma unroll
    for (int j = 0; j < 16; ++j) s += (j < cnt) ? v[j] : 0.f;
    const float mean = wsum(s) * (1.f / DM);
    float q = 0.f;
#pragma unroll
    for (int j = 0; j < 16; ++j) { const float d = v[j] - mean; q += (j < cnt) ? d * d : 0.f; }
    const float var = wsum(q) * (1.f / DM);
    const float mb = rbf(mean), sd = rbf(sqrtf(rbf(rbf(var) + 1e-5f)));
#pragma unroll
    for (int j = 0; j < 16; ++j) v[j] = rbf(rbf(v[j] - mb) / sd);
}
template <bool DOUBLE>
__global__ __launch_bounds__(256) void ln_k(const u16* __restrict__ X, u16* __restrict__ Y, u16* __restrict__ Z, u16* __restrict__ pad) {
    const int lane = threadIdx.x & 63, row = blockIdx.x * 4 + (threadIdx.x >> 6);
    const int cnt = lane < 8 ? 16 : 8;
    float v[16];
    bf16x8 t0 = *reinterpret_cast<const bf16x8*>(X + (size_t)row * DM + lane * 8);
    bf16x8 t1 = t0;
    if (lane < 8) t1 = *reinterpret_cast<const bf16x8*>(X + (size_t)row * DM + 512 + lane * 8);
#pragma unroll
    for (int j = 0; j < 8; ++j) { v[j] = bf2f((u16)t0[j]); v[8 + j] = bf2f((u16)t1[j]); }
    ln_regs(v, cnt);
#pragma unroll
    for (int j = 0; j < 8; ++j) { t0[j] = (short)f2bf(v[j]); t1[j] = (short)f2bf(v[8 + j]); }
    *reinterpret_cast<bf16x8*>(Y + (size_t)row * DM + lane * 8) = t0;
    if (lane < 8) *reinterpret_cast<bf16x8*>(Y + (size_t)row * DM + 512 + lane * 8) = t1;
    if constexpr (DOUBLE) {
        ln_regs(v, cnt);
#pragma unroll
        for (int j = 0; j < 8; ++j) { t0[j] = (short)f2bf(v[j]); t1[j] = (short)f2bf(v[8 + j]); }
        *reinterpret_cast<bf16x8*>(Z + (size_t)row * DM + lane * 8) = t0;
        if (lane < 8) *reinterpret_cast<bf16x8*>(Z + (size_t)row * DM + 512 + lane * 8) = t1;
        if (blockIdx.x == 0) {
            for (int i = threadIdx.x; i < 2 * DM; i += 256) { pad[i] = 0; pad[(size_t)(SEQ + 2) * DM + i] = 0; }
        }
    }
}

static constexpr int KC = 128;
static constexpr int KSTR = 40;
static constexpr int QKVS = 3 * DM;

__device__ __forceinline__ float max3f(float a, float b, float c) { return __builtin_fmaxf(__builtin_fmaxf(a, b), c); }
__global__ __launch_bounds__(512, 4) void attn_k(const u16* __restrict__ qkv, u16* __restrict__ O) {
    __shared__ __attribute__((aligned(16))) u16 sKV[2][2][2][KC * KSTR];
    const int grp = threadIdx.x >> 8;
    u16 (*sK)[KC * KSTR] = sKV[grp][0];
    u16 (*sV)[KC * KSTR] = sKV[grp][1];
    const int tid = threadIdx.x & 255, lane = tid & 63, wid = tid >> 6;
    const int r32 = lane & 31, hi = lane >> 5;
    const int hh = blockIdx.y;
    const int q0 = blockIdx.x * 128 + wid * 32;

    for (int i = tid; i < 2 * KC * 7; i += 256) {
        const int b = i / (KC * 7), rem = i % (KC * 7), r = rem / 7, c = 18 + (rem % 7) * 2;
        *reinterpret_cast<unsigned*>(&sK[b][r * KSTR + c]) = 0u;
        *reinterpret_cast<unsigned*>(&sV[b][r * KSTR + c]) = (c == 18) ? 0x3F80u : 0u;
    }
    bf16x8 qf[2];
    {
        const unsigned* qp = reinterpret_cast<const unsigned*>(qkv + (size_t)(q0 + r32) * QKVS + hh * DH);
        u32x4 w0 = hi ? u32x4{qp[4], qp[5], qp[6], qp[7]} : u32x4{qp[0], qp[1], qp[2], qp[3]};
        u32x4 w1 = hi ? u32x4{0u, 0u, 0u, 0u} : u32x4{qp[8], 0u, 0u, 0u};
        qf[0] = __builtin_bit_cast(bf16x8, w0);
        qf[1] = __builtin_bit_cast(bf16x8, w1);
    }
    const float C2 = 0.23570226039551584f * 1.4426950408889634f;
    float mrun = -INFINITY;
    f32x16 o = f32x16{};

    unsigned rkA[5], rvA[5], rkB[5], rvB[5];
    auto load_tiles = [&](int k0, unsigned (&rk)[5], unsigned (&rv)[5]) {
#pragma unroll
        for (int i = 0; i < 5; ++i) {
            const int idx = tid + i * 256;
            if (idx < KC * 9) {
                const int r = idx / 9, c = idx % 9;
                const unsigned* p = reinterpret_cast<const unsigned*>(qkv + (size_t)(k0 + r) * QKVS + DM + hh * DH) + c;
                rk[i] = p[0];
                rv[i] = p[DM / 2];
            }
        }
    };
    auto store_tiles = [&](int buf, unsigned (&rk)[5], unsigned (&rv)[5]) {
#pragma unroll
        for (int i = 0; i < 5; ++i) {
            const int idx = tid + i * 256;
            if (idx < KC * 9) {
                const int r = idx / 9, c = idx % 9;
                *reinterpret_cast<unsigned*>(&sK[buf][r * KSTR + c * 2]) = rk[i];
                *reinterpret_cast<unsigned*>(&sV[buf][r * KSTR + c * 2]) = rv[i];
            }
        }
    };

    constexpr int NT = SEQ / KC / 2;
    load_tiles(grp * KC, rkA, rvA);
    load_tiles((2 + grp) * KC, rkB, rvB);
    store_tiles(0, rkA, rvA);
    __syncthreads();
#pragma unroll 2
    for (int t = 0; t < NT; ++t) {
        const int buf = t & 1;
        if (t + 2 < NT) { if (buf == 0) load_tiles((2 * (t + 2) + grp) * KC, rkA, rvA); else load_tiles((2 * (t + 2) + grp) * KC, rkB, rvB); }
#pragma unroll
        for (int c = 0; c < KC / 64; ++c) {
            f32x16 s0 = f32x16{}, s1 = f32x16{};
            {
                const u16* kb = &sK[buf][(c * 64 + r32) * KSTR + hi * 8];
                bf16x8 k00 = *reinterpret_cast<const bf16x8*>(kb);
                bf16x8 k01 = *reinterpret_cast<const bf16x8*>(kb + 16);
                bf16x8 k10 = *reinterpret_cast<const bf16x8*>(kb + 32 * KSTR);
                bf16x8 k11 = *reinterpret_cast<const bf16x8*>(kb + 32 * KSTR + 16);
                s0 = __builtin_amdgcn_mfma_f32_32x32x16_bf16(k00, qf[0], s0, 0, 0, 0);
                s1 = __builtin_amdgcn_mfma_f32_32x32x16_bf16(k10, qf[0], s1, 0, 0, 0);
                s0 = __builtin_amdgcn_mfma_f32_32x32x16_bf16(k01, qf[1], s0, 0, 0, 0);
                s1 = __builtin_amdgcn_mfma_f32_32x32x16_bf16(k11, qf[1], s1, 0, 0, 0);
            }
            float tma = max3f(s0[0], s1[0], s0[1]), tmb = max3f(s1[1], s0[2], s1[2]);
#pragma unroll
            for (int r = 3; r < 15; r += 2) { tma = max3f(tma, s0[r], s1[r]); tmb = max3f(tmb, s0[r + 1], s1[r + 1]); }
            float tm = max3f(tma, tmb, max3f(s0[15], s1[15], mrun));
            {
                auto rr = __builtin_amdgcn_permlane32_swap(__float_as_uint(tm), __float_as_uint(tm), false, false);
                tm = max3f(__uint_as_float(rr[0]), __uint_as_float(rr[1]), mrun);
            }
            if (!__all(tm <= mrun)) {
                const float mnew = tm;
                const float alpha = __builtin_amdgcn_exp2f((mrun - mnew) * C2);
                mrun = mnew;
#pragma unroll
                for (int r = 0; r < 16; ++r) o[r] *= alpha;
            }
            const float nm = -mrun * C2;
#pragma unroll
            for (int r = 0; r < 16; ++r) {
                s0[r] = __builtin_amdgcn_exp2f(fmaf(s0[r], C2, nm));
                s1[r] = __builtin_amdgcn_exp2f(fmaf(s1[r], C2, nm));
            }
#pragma unroll
            for (int ks = 0; ks < 4; ++ks) {
                u32x4 pw;
#pragma unroll
                for (int j = 0; j < 4; ++j) {
                    const float a0 = (ks < 2) ? s0[(ks & 1) * 8 + 2 * j] : s1[(ks & 1) * 8 + 2 * j];
                    const float a1 = (ks < 2) ? s0[(ks & 1) * 8 + 2 * j + 1] : s1[(ks & 1) * 8 + 2 * j + 1];
                    typedef __bf16 bf2v __attribute__((ext_vector_type(2)));
                    bf2v t2; t2[0] = (__bf16)a0; t2[1] = (__bf16)a1;
                    pw[j] = __builtin_bit_cast(unsigned, t2);
                }
                const bf16x8 pf = __builtin_bit_cast(bf16x8, pw);
                const int q = (lane & 15) >> 2, p = lane & 3, cb = ((lane >> 4) & 1) * 16 + 4 * p;
                const u16* vb = &sV[buf][(c * 64 + ks * 16 + 4 * hi + q) * KSTR + cb];
                s16x4 lo = __builtin_bit_cast(s16x4, __builtin_amdgcn_ds_read_tr16_b64_v4i16((__attribute__((address_space(3))) v4i16_t*)vb));
                s16x4 hi4 = __builtin_bit_cast(s16x4, __builtin_amdgcn_ds_read_tr16_b64_v4i16((__attribute__((address_space(3))) v4i16_t*)(vb + 8 * KSTR)));
                bf16x8 vf = bf16x8{lo[0], lo[1], lo[2], lo[3], hi4[0], hi4[1], hi4[2], hi4[3]};
                o = __builtin_amdgcn_mfma_f32_32x32x16_bf16(vf, pf, o, 0, 0, 0);
            }
        }
        if (t + 1 < NT) { if (buf == 0) store_tiles(1, rkB, rvB); else store_tiles(0, rkA, rvA); }
        __syncthreads();
    }
    {
        float* sc = reinterpret_cast<float*>(&sKV[0][0][0][0]);
        if (grp == 1) {
            sc[tid] = mrun;
#pragma unroll
            for (int r = 0; r < 16; ++r) sc[(r + 1) * 256 + tid] = o[r];
        }
        __syncthreads();
        if (grp == 1) return;
        const float m2 = sc[tid];
        const float mn = fmaxf(mrun, m2);
        const float a1 = __builtin_amdgcn_exp2f((mrun - mn) * C2), a2 = __builtin_amdgcn_exp2f((m2 - mn) * C2);
#pragma unroll
        for (int r = 0; r < 16; ++r) o[r] = o[r] * a1 + sc[(r + 1) * 256 + tid] * a2;
    }
    const float l = __shfl(o[10], r32);
    const float inv = 1.f / l;
    u16* orow = O + (size_t)(q0 + r32) * DM + hh * DH;
#pragma unroll
    for (int r = 0; r < 16; ++r) {
        const int d = (r & 3) + 8 * (r >> 2) + 4 * hi;
        if (d < DH) orow[d] = f2bf(o[r] * inv);
    }
}

extern "C" void kernel_launch(void* const* d_in, const int* in_sizes, int n_in,
                              void* d_out, int out_size, void* d_ws, size_t ws_size,
                              hipStream_t stream) {
    const u16* x = (const u16*)d_in[0];
    const u16* wqkv = (const u16*)d_in[1];
    const u16* wo = (const u16*)d_in[2];
    const u16* w1 = (const u16*)d_in[4];
    const u16* w2 = (const u16*)d_in[5];
    const u16* w3 = (const u16*)d_in[6];
    const u16* w4 = (const u16*)d_in[7];
    u16* out = (u16*)d_out;

    const size_t T = (size_t)SEQ * DM;
    u16* ws = (u16*)d_ws;
    u16* qkv = ws;
    u16* xn = qkv + 3 * T;
    u16* Ob = xn + T;
    u16* hb = Ob + T;
    u16* yb = hb + T;
    u16* zb = yb + T;
    u16* hmp = zb + T;
    u16* hm = hmp + 2 * DM;
    u16* x1 = hmp + T + 4 * DM;
    u16* x1n = x1 + T;
    u16* gb = x1n + T;

    ln_k<false><<<SEQ / 4, 256, 0, stream>>>(x, xn, nullptr, nullptr);
    gemm_k<128, 64, 3, EPI_PLAIN, false><<<dim3(SEQ / 128, 3 * DM / 64), 256, 0, stream>>>(GemmArgs{xn, wqkv, qkv, nullptr, nullptr, 3 * DM, DM});
    attn_k<<<dim3(SEQ / 128, NH), 512, 0, stream>>>(qkv, Ob);
    gemm_k<64, 64, 3, EPI_RES, false><<<dim3(SEQ / 64, DM / 64), 256, 0, stream>>>(GemmArgs{Ob, wo, hb, x, nullptr, DM, DM});
    ln_k<true><<<SEQ / 4, 256, 0, stream>>>(hb, yb, zb, hmp);
    gemm_k<64, 64, 3, EPI_GELU, false><<<dim3(SEQ / 64, DM / 64), 256, 0, stream>>>(GemmArgs{zb, w1, hm, nullptr, nullptr, DM, DM});
    gemm_k<64, 64, 3, EPI_RES, true><<<dim3(SEQ / 64, DM / 64), 256, 0, stream>>>(GemmArgs{hm, w2, x1, yb, nullptr, DM, DM});
    ln_k<false><<<SEQ / 4, 256, 0, stream>>>(x1, x1n, nullptr, nullptr);
    gemm_k<128, 192, 3, EPI_GELU, false><<<dim3(SEQ / 128, FF / 192), 256, 0, stream>>>(GemmArgs{x1n, w3, gb, nullptr, nullptr, FF, DM});
    u16* pk = gb + (size_t)SEQ * FF;
    gemm_k<128, 192, 2, EPI_PART, false><<<dim3(SEQ / 128, DM / 192, SPLITK), 256, 0, stream>>>(GemmArgs{gb, w4, pk, nullptr, nullptr, DM, FF});
    redk_k<<<SEQ * DM / 1024, 256, 0, stream>>>((const float*)pk, x1, hb, out);
}
```

```cpp
#include <hip/hip_runtime.h>
#include <hip/hip_bf16.h>
#include <stdint.h>

static constexpr int SEQ = 2048, DM = 576, NH = 32, DH = 18, FF = 4608;
static constexpr int DP = 32;

using bf16x8 = __attribute__((ext_vector_type(8))) short;
using s16x4 = __attribute__((ext_vector_type(4))) short;
using f32x4 = __attribute__((ext_vector_type(4))) float;
using f32x16 = __attribute__((ext_vector_type(16))) float;
using u32x4 = __attribute__((ext_vector_type(4))) unsigned;
typedef short v4i16_t __attribute__((ext_vector_type(4)));
typedef unsigned short u16;

__device__ __forceinline__ float bf2f(u16 v) { return __uint_as_float(((unsigned)v) << 16); }
__device__ __forceinline__ u16 f2bf(float f) {
    unsigned u = __float_as_uint(f);
    u += 0x7FFF + ((u >> 16) & 1);
    return (u16)(u >> 16);
}
__device__ __forceinline__ float rbf(float f) { return bf2f(f2bf(f)); }

__device__ __forceinline__ float gelu_tanh(float x) {
    const float u = 0.7978845608028654f * (x + 0.044715f * x * x * x);
    const float e = __expf(2.f * u);
    const float t = 1.f - 2.f / (e + 1.f);
    return 0.5f * x * (1.f + t);
}


struct GemmArgs {
    const u16* A;
    const u16* B;
    u16* C;
    const u16* R1;
    const u16* R2;
    int N;
    int K;
};
enum { EPI_PLAIN = 0, EPI_RES = 1, EPI_GELU = 2, EPI_RES2 = 3 };

template <int BN>
__device__ __forceinline__ int swzB(int row) {
    if constexpr (BN == 64) return ((((row >> 1) & 1) | (((row >> 3) & 1) << 1)) << 1);
    else return (((row & 3) << 1) | (((row >> 3) & 1) << 3));
}

template <int BM, int BN, int NS, int EPI, bool SHIFT>
__global__ __launch_bounds__(256, 2) void gemm_k(GemmArgs a) {
    constexpr int BK = 64;
    constexpr int WM = BM / 2, WN = BN / 2, TM = WM / 16, TN = WN / 16;
    constexpr int ACH = BM * 8 / 256, BCH = BK * BN / 8 / 256, L = ACH + BCH;
    constexpr int ASZ = BM * BK, BSZ = BK * BN, STG = ASZ + BSZ;
    constexpr int CSTR = BN + 8;
    static_assert(BM * CSTR <= NS * STG, "epilogue tile must fit");
    __shared__ __attribute__((aligned(16))) u16 smem[NS * STG];

    const int tid = threadIdx.x, lane = tid & 63, wid = tid >> 6;
    const int wm = wid >> 1, wn = wid & 1;
    const int lb = blockIdx.y * gridDim.x + blockIdx.x, jj = lb >> 3;
    const int m0 = ((lb & 7) + 8 * (jj / (int)gridDim.y)) * BM, n0 = (jj % (int)gridDim.y) * BN;
    const int K = a.K, N = a.N, nk = K / BK;

    const u16* asrc[ACH];
    const u16* bsrc[BCH];
#pragma unroll
    for (int i = 0; i < ACH; ++i) {
        const int idx = tid + i * 256, row = idx >> 3, ch = (idx & 7) ^ (row & 7);
        asrc[i] = a.A + (size_t)(m0 + row) * K + ch * 8;
    }
#pragma unroll
    for (int i = 0; i < BCH; ++i) {
        const int idx = tid + i * 256, row = idx / (BN / 8), ch = (idx % (BN / 8)) ^ swzB<BN>(row);
        bsrc[i] = a.B + (size_t)row * N + n0 + ch * 8;
    }
    auto issue = [&](int kt, int slot) {
        u16* st = smem + slot * STG;
#pragma unroll
        for (int i = 0; i < ACH; ++i) {
            long off = (long)kt * BK;
            if constexpr (SHIFT) {
                const int col = kt * BK + (((tid + i * 256) & 7) ^ (((tid + i * 256) >> 3) & 7)) * 8;
                const int grp = col / 144;
                off += (long)K * ((grp == 0) ? -1 : (grp == 1) ? 1 : (grp == 2) ? -2 : 2);
            }
            __builtin_amdgcn_global_load_lds((const unsigned*)(asrc[i] + off), (unsigned*)(st + (tid + i * 256) * 8), 16, 0, 0);
        }
#pragma unroll
        for (int i = 0; i < BCH; ++i)
            __builtin_amdgcn_global_load_lds((const unsigned*)(bsrc[i] + (size_t)kt * BK * N), (unsigned*)(st + ASZ + (tid + i * 256) * 8), 16, 0, 0);
    };

    f32x4 acc[TM][TN];
#pragma unroll
    for (int i = 0; i < TM; ++i)
#pragma unroll
        for (int j = 0; j < TN; ++j) acc[i][j] = f32x4{0.f, 0.f, 0.f, 0.f};

#pragma unroll
    for (int s = 0; s < NS - 1; ++s) if (s < nk) issue(s, s);

    for (int kt0 = 0; kt0 < nk; kt0 += NS)
#pragma unroll
    for (int sl = 0; sl < NS; ++sl) {
        const int kt = kt0 + sl;
        if (kt + NS - 2 < nk) asm volatile("s_waitcnt vmcnt(%0) lgkmcnt(0)" :: "i"((NS - 2) * L) : "memory");
        else asm volatile("s_waitcnt vmcnt(0) lgkmcnt(0)" ::: "memory");
        __builtin_amdgcn_s_barrier();
        asm volatile("" ::: "memory");
        if (kt + NS - 1 < nk) issue(kt + NS - 1, (sl + NS - 1) % NS);
        const u16* sA = smem + sl * STG;
        const u16* sB = sA + ASZ;
#pragma unroll
        for (int kk = 0; kk < 2; ++kk) {
            bf16x8 af[TM], bfr[TN];
            s16x4 blo[TN], bhi[TN];
#pragma unroll
            for (int i = 0; i < TM; ++i) {
                const int row = wm * WM + i * 16 + (lane & 15);
                af[i] = *reinterpret_cast<const bf16x8*>(sA + row * BK + ((kk * 4 + (lane >> 4)) ^ (row & 7)) * 8);
            }
#pragma unroll
            for (int j = 0; j < TN; ++j) {
                const int g = lane >> 4, q = (lane & 15) >> 2, p = lane & 3;
                const int r0 = kk * 32 + 8 * g + q, r1 = r0 + 4;
                const int ch = (wn * WN + j * 16) / 8 + (p >> 1);
                const u16* p0 = sB + r0 * BN + (ch ^ swzB<BN>(r0)) * 8 + (p & 1) * 4;
                const u16* p1 = sB + r1 * BN + (ch ^ swzB<BN>(r1)) * 8 + (p & 1) * 4;
                s16x4 lo, hi;
                asm volatile("ds_read_b64_tr_b16 %0, %1" : "=v"(lo) : "v"((unsigned)(uintptr_t)p0) : "memory");
                asm volatile("ds_read_b64_tr_b16 %0, %1" : "=v"(hi) : "v"((unsigned)(uintptr_t)p1) : "memory");
                blo[j] = lo; bhi[j] = hi;
            }
#pragma unroll
            for (int j = 0; j < TN; ++j) asm volatile("s_waitcnt lgkmcnt(0)" : "+v"(blo[j]), "+v"(bhi[j]) :: "memory");
#pragma unroll
            for (int j = 0; j < TN; ++j) {
                s16x4 lo = blo[j], hi = bhi[j];
                bfr[j] = bf16x8{lo[0], lo[1], lo[2], lo[3], hi[0], hi[1], hi[2], hi[3]};
            }
#pragma unroll
            for (int i = 0; i < TM; ++i)
#pragma unroll
                for (int j = 0; j < TN; ++j)
                    acc[i][j] = __builtin_amdgcn_mfma_f32_16x16x32_bf16(af[i], bfr[j], acc[i][j], 0, 0, 0);
        }
    }
    __syncthreads();
#pragma unroll
    for (int i = 0; i < TM; ++i)
#pragma unroll
        for (int j = 0; j < TN; ++j)
#pragma unroll
            for (int r = 0; r < 4; ++r)
                smem[(wm * WM + i * 16 + (lane >> 4) * 4 + r) * CSTR + wn * WN + j * 16 + (lane & 15)] = f2bf(acc[i][j][r]);
    __syncthreads();
    constexpr int CCH = BM * BN / 8 / 256;
#pragma unroll
    for (int i = 0; i < CCH; ++i) {
        const int idx = tid + i * 256, row = idx / (BN / 8), ch = idx % (BN / 8);
        bf16x8 v = *reinterpret_cast<const bf16x8*>(smem + row * CSTR + ch * 8);
        const size_t go = (size_t)(m0 + row) * N + n0 + ch * 8;
        if constexpr (EPI == EPI_RES || EPI == EPI_RES2) {
            bf16x8 r1 = *reinterpret_cast<const bf16x8*>(a.R1 + go);
#pragma unroll
            for (int j = 0; j < 8; ++j) v[j] = (short)f2bf(bf2f((u16)r1[j]) + bf2f((u16)v[j]));
            if constexpr (EPI == EPI_RES2) {
                bf16x8 r2 = *reinterpret_cast<const bf16x8*>(a.R2 + go);
#pragma unroll
                for (int j = 0; j < 8; ++j) v[j] = (short)f2bf(bf2f((u16)r2[j]) + bf2f((u16)v[j]));
            }
        } else if constexpr (EPI == EPI_GELU) {
#pragma unroll
            for (int j = 0; j < 8; ++j) v[j] = (short)f2bf(gelu_tanh(bf2f((u16)v[j])));
        }
        *reinterpret_cast<bf16x8*>(a.C + go) = v;
    }
}

__device__ __forceinline__ float wsum(float v) {
#pragma unroll
    for (int o = 32; o > 0; o >>= 1) v += __shfl_xor(v, o);
    return v;
}
__device__ __forceinline__ void ln_regs(float (&v)[16], int cnt) {
    float s = 0.f;
#pragma unroll
    for (int j = 0; j < 16; ++j) s += (j < cnt) ? v[j] : 0.f;
    const float mean = wsum(s) * (1.f / DM);
    float q = 0.f;
#pragma unroll
    for (int j = 0; j < 16; ++j) { const float d = v[j] - mean; q += (j < cnt) ? d * d : 0.f; }
    const float var = wsum(q) * (1.f / DM);
    const float mb = rbf(mean), sd = rbf(sqrtf(rbf(rbf(var) + 1e-5f)));
#pragma unroll
    for (int j = 0; j < 16; ++j) v[j] = rbf(rbf(v[j] - mb) / sd);
}
template <bool DOUBLE>
__global__ __launch_bounds__(256) void ln_k(const u16* __restrict__ X, u16* __restrict__ Y, u16* __restrict__ Z, u16* __restrict__ pad) {
    const int lane = threadIdx.x & 63, row = blockIdx.x * 4 + (threadIdx.x >> 6);
    const int cnt = lane < 8 ? 16 : 8;
    float v[16];
    bf16x8 t0 = *reinterpret_cast<const bf16x8*>(X + (size_t)row * DM + lane * 8);
    bf16x8 t1 = t0;
    if (lane < 8) t1 = *reinterpret_cast<const bf16x8*>(X + (size_t)row * DM + 512 + lane * 8);
#pragma unroll
    for (int j = 0; j < 8; ++j) { v[j] = bf2f((u16)t0[j]); v[8 + j] = bf2f((u16)t1[j]); }
    ln_regs(v, cnt);
#pragma unroll
    for (int j = 0; j < 8; ++j) { t0[j] = (short)f2bf(v[j]); t1[j] = (short)f2bf(v[8 + j]); }
    *reinterpret_cast<bf16x8*>(Y + (size_t)row * DM + lane * 8) = t0;
    if (lane < 8) *reinterpret_cast<bf16x8*>(Y + (size_t)row * DM + 512 + lane * 8) = t1;
    if constexpr (DOUBLE) {
        ln_regs(v, cnt);
#pragma unroll
        for (int j = 0; j < 8; ++j) { t0[j] = (short)f2bf(v[j]); t1[j] = (short)f2bf(v[8 + j]); }
        *reinterpret_cast<bf16x8*>(Z + (size_t)row * DM + lane * 8) = t0;
        if (lane < 8) *reinterpret_cast<bf16x8*>(Z + (size_t)row * DM + 512 + lane * 8) = t1;
        if (blockIdx.x == 0) {
            for (int i = threadIdx.x; i < 2 * DM; i += 256) { pad[i] = 0; pad[(size_t)(SEQ + 2) * DM + i] = 0; }
        }
    }
}

static constexpr int KC = 128;
static constexpr int KSTR = 40;
static constexpr int QKVS = 3 * DM;

__device__ __forceinline__ float max3f(float a, float b, float c) { return __builtin_fmaxf(__builtin_fmaxf(a, b), c); }
__global__ __launch_bounds__(512, 4) void attn_k(const u16* __restrict__ qkv, u16* __restrict__ O) {
    __shared__ __attribute__((aligned(16))) u16 sKV[2][2][2][KC * KSTR];
    const int grp = threadIdx.x >> 8;
    u16 (*sK)[KC * KSTR] = sKV[grp][0];
    u16 (*sV)[KC * KSTR] = sKV[grp][1];
    const int tid = threadIdx.x & 255, lane = tid & 63, wid = tid >> 6;
    const int r32 = lane & 31, hi = lane >> 5;
    const int hh = blockIdx.y;
    const int q0 = blockIdx.x * 128 + wid * 32;

    for (int i = tid; i < 2 * KC * 7; i += 256) {
        const int b = i / (KC * 7), rem = i % (KC * 7), r = rem / 7, c = 18 + (rem % 7) * 2;
        *reinterpret_cast<unsigned*>(&sK[b][r * KSTR + c]) = 0u;
        *reinterpret_cast<unsigned*>(&sV[b][r * KSTR + c]) = (c == 18) ? 0x3F80u : 0u;
    }
    bf16x8 qf[2];
    {
        const unsigned* qp = reinterpret_cast<const unsigned*>(qkv + (size_t)(q0 + r32) * QKVS + hh * DH);
        u32x4 w0 = hi ? u32x4{qp[4], qp[5], qp[6], qp[7]} : u32x4{qp[0], qp[1], qp[2], qp[3]};
        u32x4 w1 = hi ? u32x4{0u, 0u, 0u, 0u} : u32x4{qp[8], 0u, 0u, 0u};
        qf[0] = __builtin_bit_cast(bf16x8, w0);
        qf[1] = __builtin_bit_cast(bf16x8, w1);
    }
    const float C2 = 0.23570226039551584f * 1.4426950408889634f;
    float mrun = -INFINITY;
    f32x16 o = f32x16{};

    unsigned rkA[5], rvA[5], rkB[5], rvB[5];
    auto load_tiles = [&](int k0, unsigned (&rk)[5], unsigned (&rv)[5]) {
#pragma unroll
        for (int i = 0; i < 5; ++i) {
            const int idx = tid + i * 256;
            if (idx < KC * 9) {
                const int r = idx / 9, c = idx % 9;
                const unsigned* p = reinterpret_cast<const unsigned*>(qkv + (size_t)(k0 + r) * QKVS + DM + hh * DH) + c;
                rk[i] = p[0];
                rv[i] = p[DM / 2];
            }
        }
    };
    auto store_tiles = [&](int buf, unsigned (&rk)[5], unsigned (&rv)[5]) {
#pragma unroll
        for (int i = 0; i < 5; ++i) {
            const int idx = tid + i * 256;
            if (idx < KC * 9) {
                const int r = idx / 9, c = idx % 9;
                *reinterpret_cast<unsigned*>(&sK[buf][r * KSTR + c * 2]) = rk[i];
                *reinterpret_cast<unsigned*>(&sV[buf][r * KSTR + c * 2]) = rv[i];
            }
        }
    };

    constexpr int NT = SEQ / KC / 2;
    load_tiles(grp * KC, rkA, rvA);
    load_tiles((2 + grp) * KC, rkB, rvB);
    store_tiles(0, rkA, rvA);
    __syncthreads();
#pragma unroll 2
    for (int t = 0; t < NT; ++t) {
        const int buf = t & 1;
        if (t + 2 < NT) { if (buf == 0) load_tiles((2 * (t + 2) + grp) * KC, rkA, rvA); else load_tiles((2 * (t + 2) + grp) * KC, rkB, rvB); }
#pragma unroll
        for (int c = 0; c < KC / 64; ++c) {
            f32x16 s0 = f32x16{}, s1 = f32x16{};
            {
                const u16* kb = &sK[buf][(c * 64 + r32) * KSTR + hi * 8];
                bf16x8 k00 = *reinterpret_cast<const bf16x8*>(kb);
                bf16x8 k01 = *reinterpret_cast<const bf16x8*>(kb + 16);
                bf16x8 k10 = *reinterpret_cast<const bf16x8*>(kb + 32 * KSTR);
                bf16x8 k11 = *reinterpret_cast<const bf16x8*>(kb + 32 * KSTR + 16);
                s0 = __builtin_amdgcn_mfma_f32_32x32x16_bf16(k00, qf[0], s0, 0, 0, 0);
                s1 = __builtin_amdgcn_mfma_f32_32x32x16_bf16(k10, qf[0], s1, 0, 0, 0);
                s0 = __builtin_amdgcn_mfma_f32_32x32x16_bf16(k01, qf[1], s0, 0, 0, 0);
                s1 = __builtin_amdgcn_mfma_f32_32x32x16_bf16(k11, qf[1], s1, 0, 0, 0);
            }
            float tma = max3f(s0[0], s1[0], s0[1]), tmb = max3f(s1[1], s0[2], s1[2]);
#pragma unroll
            for (int r = 3; r < 15; r += 2) { tma = max3f(tma, s0[r], s1[r]); tmb = max3f(tmb, s0[r + 1], s1[r + 1]); }
            float tm = max3f(tma, tmb, max3f(s0[15], s1[15], mrun));
            {
                auto rr = __builtin_amdgcn_permlane32_swap(__float_as_uint(tm), __float_as_uint(tm), false, false);
                tm = max3f(__uint_as_float(rr[0]), __uint_as_float(rr[1]), mrun);
            }
            if (!__all(tm <= mrun)) {
                const float mnew = tm;
                const float alpha = __builtin_amdgcn_exp2f((mrun - mnew) * C2);
                mrun = mnew;
#pragma unroll
                for (int r = 0; r < 16; ++r) o[r] *= alpha;
            }
            const float nm = -mrun * C2;
#pragma unroll
            for (int r = 0; r < 16; ++r) {
                s0[r] = __builtin_amdgcn_exp2f(fmaf(s0[r], C2, nm));
                s1[r] = __builtin_amdgcn_exp2f(fmaf(s1[r], C2, nm));
            }
#pragma unroll
            for (int ks = 0; ks < 4; ++ks) {
                u32x4 pw;
#pragma unroll
                for (int j = 0; j < 4; ++j) {
                    const float a0 = (ks < 2) ? s0[(ks & 1) * 8 + 2 * j] : s1[(ks & 1) * 8 + 2 * j];
                    const float a1 = (ks < 2) ? s0[(ks & 1) * 8 + 2 * j + 1] : s1[(ks & 1) * 8 + 2 * j + 1];
                    typedef __bf16 bf2v __attribute__((ext_vector_type(2)));
                    bf2v t2; t2[0] = (__bf16)a0; t2[1] = (__bf16)a1;
                    pw[j] = __builtin_bit_cast(unsigned, t2);
                }
                const bf16x8 pf = __builtin_bit_cast(bf16x8, pw);
                const int q = (lane & 15) >> 2, p = lane & 3, cb = ((lane >> 4) & 1) * 16 + 4 * p;
                const u16* vb = &sV[buf][(c * 64 + ks * 16 + 4 * hi + q) * KSTR + cb];
                s16x4 lo = __builtin_bit_cast(s16x4, __builtin_amdgcn_ds_read_tr16_b64_v4i16((__attribute__((address_space(3))) v4i16_t*)vb));
                s16x4 hi4 = __builtin_bit_cast(s16x4, __builtin_amdgcn_ds_read_tr16_b64_v4i16((__attribute__((address_space(3))) v4i16_t*)(vb + 8 * KSTR)));
                bf16x8 vf = bf16x8{lo[0], lo[1], lo[2], lo[3], hi4[0], hi4[1], hi4[2], hi4[3]};
                o = __builtin_amdgcn_mfma_f32_32x32x16_bf16(vf, pf, o, 0, 0, 0);
            }
        }
        if (t + 1 < NT) { if (buf == 0) store_tiles(1, rkB, rvB); else store_tiles(0, rkA, rvA); }
        __syncthreads();
    }
    {
        float* sc = reinterpret_cast<float*>(&sKV[0][0][0][0]);
        if (grp == 1) {
            sc[tid] = mrun;
#pragma unroll
            for (int r = 0; r < 16; ++r) sc[(r + 1) * 256 + tid] = o[r];
        }
        __syncthreads();
        if (grp == 1) return;
        const float m2 = sc[tid];
        const float mn = fmaxf(mrun, m2);
        const float a1 = __builtin_amdgcn_exp2f((mrun - mn) * C2), a2 = __builtin_amdgcn_exp2f((m2 - mn) * C2);
#pragma unroll
        for (int r = 0; r < 16; ++r) o[r] = o[r] * a1 + sc[(r + 1) * 256 + tid] * a2;
    }
    const float l = __shfl(o[10], r32);
    const float inv = 1.f / l;
    u16* orow = O + (size_t)(q0 + r32) * DM + hh * DH;
#pragma unroll
    for (int r = 0; r < 16; ++r) {
        const int d = (r & 3) + 8 * (r >> 2) + 4 * hi;
        if (d < DH) orow[d] = f2bf(o[r] * inv);
    }
}

extern "C" void kernel_launch(void* const* d_in, const int* in_sizes, int n_in,
                              void* d_out, int out_size, void* d_ws, size_t ws_size,
                              hipStream_t stream) {
    const u16* x = (const u16*)d_in[0];
    const u16* wqkv = (const u16*)d_in[1];
    const u16* wo = (const u16*)d_in[2];
    const u16* w1 = (const u16*)d_in[4];
    const u16* w2 = (const u16*)d_in[5];
    const u16* w3 = (const u16*)d_in[6];
    const u16* w4 = (const u16*)d_in[7];
    u16* out = (u16*)d_out;

    const size_t T = (size_t)SEQ * DM;
    u16* ws = (u16*)d_ws;
    u16* qkv = ws;
    u16* xn = qkv + 3 * T;
    u16* Ob = xn + T;
    u16* hb = Ob + T;
    u16* yb = hb + T;
    u16* zb = yb + T;
    u16* hmp = zb + T;
    u16* hm = hmp + 2 * DM;
    u16* x1 = hmp + T + 4 * DM;
    u16* x1n = x1 + T;
    u16* gb = x1n + T;

    ln_k<false><<<SEQ / 4, 256, 0, stream>>>(x, xn, nullptr, nullptr);
    gemm_k<128, 64, 3, EPI_PLAIN, false><<<dim3(SEQ / 128, 3 * DM / 64), 256, 0, stream>>>(GemmArgs{xn, wqkv, qkv, nullptr, nullptr, 3 * DM, DM});
    attn_k<<<dim3(SEQ / 128, NH), 512, 0, stream>>>(qkv, Ob);
    gemm_k<64, 64, 3, EPI_RES, false><<<dim3(SEQ / 64, DM / 64), 256, 0, stream>>>(GemmArgs{Ob, wo, hb, x, nullptr, DM, DM});
    ln_k<true><<<SEQ / 4, 256, 0, stream>>>(hb, yb, zb, hmp);
    gemm_k<64, 64, 3, EPI_GELU, false><<<dim3(SEQ / 64, DM / 64), 256, 0, stream>>>(GemmArgs{zb, w1, hm, nullptr, nullptr, DM, DM});
    gemm_k<64, 64, 3, EPI_RES, true><<<dim3(SEQ / 64, DM / 64), 256, 0, stream>>>(GemmArgs{hm, w2, x1, yb, nullptr, DM, DM});
    ln_k<false><<<SEQ / 4, 256, 0, stream>>>(x1, x1n, nullptr, nullptr);
    gemm_k<128, 64, 3, EPI_GELU, false><<<dim3(SEQ / 128, FF / 64), 256, 0, stream>>>(GemmArgs{x1n, w3, gb, nullptr, nullptr, FF, DM});
    gemm_k<64, 64, 4, EPI_RES2, false><<<dim3(SEQ / 64, DM / 64), 256, 0, stream>>>(GemmArgs{gb, w4, out, x1, hb, DM, FF});
}
```
